```python
import torch

def _softmax(x, axis=-1):
    return torch.softmax(x, dim=axis)
def _gelu(x):
    return torch.nn.functional.gelu(x, approximate="tanh")
def _silu(x):
    return torch.nn.functional.silu(x)
def _sigmoid(x):
    return torch.sigmoid(x)
def _layernorm(x, axis=-1, eps=1e-5):
    m = x.mean(dim=axis, keepdim=True)
    v = x.var(dim=axis, keepdim=True, unbiased=False)
    return (x - m) / torch.sqrt(v + eps)
def _scatter_add(tgt, idx, src, axis=0):
    return tgt.clone().index_add_(axis, idx, src)
def _segment_sum(src, seg, n):
    out = torch.zeros((n,) + src.shape[1:], dtype=src.dtype)
    return out.index_add_(0, seg, src)
def _pad(x, pw):
    flat = tuple(v for pair in reversed(pw) for v in pair)
    return torch.nn.functional.pad(x, flat)

kernel_name = 'concat_heads__ssm_conv_s4__spectral_gfnet__prompt'

def setup_inputs():

    _w = (lambda a: (lambda t: t.to(torch.bfloat16) if t.is_floating_point() else t)(torch.as_tensor(a)))
    def _A_setup():
        B, L, D, N = 1, 2048, 576, 128
        A_re = -(lambda _x: torch.exp(torch.as_tensor(_x)))((lambda *s: torch.randn(*s, dtype=torch.bfloat16) * 3.2)(D, N))
        A_im = torch.pi * (lambda *s: torch.randn(*s, dtype=torch.bfloat16) * 0.05)(D, N)
        B_re = (lambda *s: torch.randn(*s, dtype=torch.bfloat16) * 0.1)(D, N)
        B_im = (lambda *s: torch.randn(*s, dtype=torch.bfloat16) * 0.1)(D, N)
        C_re = (lambda *s: torch.randn(*s, dtype=torch.bfloat16) * 0.1)(D, N)
        C_im = (lambda *s: torch.randn(*s, dtype=torch.bfloat16) * 0.1)(D, N)
        log_dt = (lambda *s: torch.randn(*s, dtype=torch.bfloat16) * 0.05)(D) * 0.5 - 4.0
        Dskip = (lambda *s: torch.randn(*s, dtype=torch.bfloat16) * 0.05)(D)
        return {
            "u": _w((lambda *s: torch.randn(*s, dtype=torch.bfloat16) * 0.05)(B, L, D)),
            "A_re": _w(A_re),
            "A_im": _w(A_im),
            "B_re": _w(B_re),
            "B_im": _w(B_im),
            "C_re": _w(C_re),
            "C_im": _w(C_im),
            "log_dt": _w(log_dt),
            "Dskip": _w(Dskip),
        }
    _A_in = _A_setup()

    def _B_setup():
        B, S, D = 1, 2048, 576
        Df = D // 2 + 1
        return {
            "x": _w((lambda *s: torch.randn(*s, dtype=torch.bfloat16) * 0.05)(B, S, D)),
            "w_r": _w((lambda *s: torch.randn(*s, dtype=torch.bfloat16) * 1.6)(S, Df) * 0.02),
            "w_i": _w((lambda *s: torch.randn(*s, dtype=torch.bfloat16) * 1.6)(S, Df) * 0.02),
        }
    _B_in = _B_setup()

    return {
        "A_u": _A_in["u"],
        "A_A_re": _A_in["A_re"],
        "A_A_im": _A_in["A_im"],
        "A_B_re": _A_in["B_re"],
        "A_B_im": _A_in["B_im"],
        "A_C_re": _A_in["C_re"],
        "A_C_im": _A_in["C_im"],
        "A_log_dt": _A_in["log_dt"],
        "A_Dskip": _A_in["Dskip"],
        "B_x": _B_in["x"],
        "B_w_r": _B_in["w_r"],
        "B_w_i": _B_in["w_i"],
    }

def reference(A_u, A_A_re, A_A_im, A_B_re, A_B_im, A_C_re, A_C_im, A_log_dt, A_Dskip, B_x, B_w_r, B_w_i):
    def _A_ref(u, A_re, A_im, B_re, B_im, C_re, C_im, log_dt, Dskip):
        Bsz, L, D = u.shape
        dt = torch.exp(log_dt)[:, None]
        A = A_re + 1j * A_im
        Bc = B_re + 1j * B_im
        Cc = C_re + 1j * C_im
        logdA = dt * A
        dB = dt * Bc
        l = (lambda x, dt: x.to(dt))(torch.arange(L), A_re.dtype)
        P = torch.exp(logdA[:, :, None] * l[None, None, :])
        K = torch.einsum("dn,dnl,dn->dl", Cc, P, dB).real
        L2 = 2 * L
        Kp = _pad(K, ((0, 0), (0, L)))
        up = (lambda x, *d: x.permute(*d))(u, 0, 2, 1)
        up = _pad(up, ((0, 0), (0, 0), (0, L)))
        Kf = (lambda x, axes=(-2,-1): torch.fft.rfft2(x.float(), dim=axes))(Kp[:, None, :], axes=(-2, -1))
        Uf = (lambda x, axes=(-2,-1): torch.fft.rfft2(x.float(), dim=axes))(up[:, :, None, :], axes=(-2, -1))
        Yf = Uf * Kf[None, :, :, :]
        y = (lambda x, s, axes=(-2,-1): torch.fft.irfft2(x, s=s, dim=axes).to(torch.bfloat16))(Yf, (1, L2), axes=(-2, -1))[:, :, 0, :L]
        y = (lambda x, *d: x.permute(*d))(y, 0, 2, 1) + u * Dskip[None, None, :]
        return y

    def _B_ref(x, w_r, w_i):
        B, S, D = x.shape
        X = (lambda x, axes=(-2,-1): torch.fft.rfft2(x.float(), dim=axes))(x, axes=(-2, -1))
        W = w_r + 1j * w_i
        X = X * W
        y = (lambda x, s, axes=(-2,-1): torch.fft.irfft2(x, s=s, dim=axes).to(torch.bfloat16))(X, (S, D), axes=(-2, -1))
        return y

    a = _A_ref(A_u, A_A_re, A_A_im, A_B_re, A_B_im, A_C_re, A_C_im, A_log_dt, A_Dskip)
    b = _B_ref(B_x, B_w_r, B_w_i)
    return torch.concatenate([a, b], axis=-1)
```

```cpp
#include <hip/hip_runtime.h>
#include <hip/hip_bf16.h>
typedef unsigned short u16;
__device__ __forceinline__ float bf2f(u16 v) { return __uint_as_float(((unsigned)v) << 16); }
__device__ __forceinline__ u16 f2bf(float f) { unsigned u = __float_as_uint(f); u += 0x7fffu + ((u >> 16) & 1u); return (u16)(u >> 16); }
constexpr int L = 2048, D = 576, N = 128, DF = 289;

__global__ void n_kgen(const u16* Are, const u16* Aim, const u16* Bre, const u16* Bim, const u16* Cre, const u16* Cim,
                       const u16* logdt, float* K) {
    __shared__ float sa[N], sb[N], scr[N], sci[N];
    __shared__ float kd[641];
    int d = blockIdx.x;
    float dt = bf2f(f2bf(expf(bf2f(logdt[d]))));
    if (threadIdx.x < N) {
        int n = threadIdx.x;
        sa[n] = dt * bf2f(Are[d * N + n]); sb[n] = dt * bf2f(Aim[d * N + n]);
        float br = dt * bf2f(Bre[d * N + n]), bi = dt * bf2f(Bim[d * N + n]);
        float cr = bf2f(Cre[d * N + n]), ci = bf2f(Cim[d * N + n]);
        scr[n] = cr * br - ci * bi; sci[n] = cr * bi + ci * br;
    }
    __syncthreads();
    for (int i = threadIdx.x; i < 641; i += blockDim.x) {
        int l = i < 256 ? i : i < 384 ? 256 + 2 * (i - 256) : i < 512 ? 512 + 4 * (i - 384) : 1024 + 8 * (i - 512);
        float lf = (float)l, acc = 0.f;
        for (int n = 0; n < N; n++) {
            float e = __expf(sa[n] * lf), ang = sb[n] * lf * 0.15915494309f;
            ang -= floorf(ang);
            float c = __builtin_amdgcn_cosf(ang), s = __builtin_amdgcn_sinf(ang);
            acc += e * (scr[n] * c - sci[n] * s);
        }
        kd[i] = acc;
    }
    __syncthreads();
    for (int l = threadIdx.x; l < L; l += blockDim.x) {
        int r = (int)bf2f(f2bf((float)l));
        int i = r < 256 ? r : r < 512 ? 256 + (r - 256) / 2 : r < 1024 ? 384 + (r - 512) / 4 : 512 + (r - 1024) / 8;
        K[d * L + l] = kd[i];
    }
}
__global__ void n_conv(const u16* u, const float* K, const u16* Dskip, u16* out) {
    __shared__ float su[L], sk[L];
    int d = blockIdx.x;
    for (int i = threadIdx.x; i < L; i += blockDim.x) { su[i] = bf2f(u[i * D + d]); sk[i] = K[d * L + i]; }
    __syncthreads();
    float ds = bf2f(Dskip[d]);
    for (int t = threadIdx.x; t < L; t += blockDim.x) {
        float acc = 0.f;
        for (int l = 0; l <= t; l++) acc += sk[l] * su[t - l];
        float y = bf2f(f2bf(acc)), z = bf2f(f2bf(su[t] * ds));
        out[t * (2 * D) + d] = f2bf(y + z);
    }
}
__global__ void n_rowdft(const u16* x, float2* X1) {
    __shared__ float sx[D]; __shared__ float2 tw[D];
    int s = blockIdx.x;
    for (int i = threadIdx.x; i < D; i += blockDim.x) { sx[i] = bf2f(x[s * D + i]); float a = (float)i / D; tw[i] = make_float2(__builtin_amdgcn_cosf(a), -__builtin_amdgcn_sinf(a)); }
    __syncthreads();
    int f = threadIdx.x;
    if (f < DF) {
        float re = 0, im = 0; int idx = 0;
        for (int d = 0; d < D; d++) { float2 t = tw[idx]; re += sx[d] * t.x; im += sx[d] * t.y; idx += f; if (idx >= D) idx -= D; }
        X1[f * L + s] = make_float2(re, im);
    }
}
__global__ void n_col(float2* X1, const u16* wr, const u16* wi) {
    __shared__ float2 a[2][L];
    int f = blockIdx.x, t = threadIdx.x;
    a[0][t] = X1[f * L + t]; a[0][t + 1024] = X1[f * L + t + 1024];
    __syncthreads();
    int cur = 0;
    for (int dir = 0; dir < 2; dir++) {
        float sg = dir ? 1.f : -1.f;
        for (int ns = 1; ns < L; ns *= 2) {
            int k = t & (ns - 1);
            float ang = (float)k / (2 * ns);
            float c = __builtin_amdgcn_cosf(ang), s = sg * __builtin_amdgcn_sinf(ang);
            float2 p = a[cur][t], q = a[cur][t + 1024];
            float2 w = make_float2(q.x * c - q.y * s, q.x * s + q.y * c);
            int o = (t - k) * 2 + k;
            a[cur ^ 1][o] = make_float2(p.x + w.x, p.y + w.y);
            a[cur ^ 1][o + ns] = make_float2(p.x - w.x, p.y - w.y);
            cur ^= 1;
            __syncthreads();
        }
        if (dir == 0) {
            for (int k = t; k < L; k += 1024) {
                float2 v = a[cur][k]; float r = bf2f(wr[k * DF + f]), i = bf2f(wi[k * DF + f]);
                a[cur][k] = make_float2(v.x * r - v.y * i, v.x * i + v.y * r);
            }
            __syncthreads();
        }
    }
    X1[f * L + t] = a[cur][t]; X1[f * L + t + 1024] = a[cur][t + 1024];
}
__global__ void n_rowidft(const float2* X1, u16* out) {
    __shared__ float2 z[DF]; __shared__ float2 tw[D];
    int s = blockIdx.x;
    for (int i = threadIdx.x; i < D; i += blockDim.x) { float a = (float)i / D; tw[i] = make_float2(__builtin_amdgcn_cosf(a), __builtin_amdgcn_sinf(a)); }
    for (int i = threadIdx.x; i < DF; i += blockDim.x) { float2 v = X1[i * L + s]; float m = (i == 0 || i == 288) ? 1.f : 2.f; z[i] = make_float2(v.x * m, (i == 0 || i == 288) ? 0.f : v.y * m); }
    __syncthreads();
    int d = threadIdx.x;
    if (d < D) {
        float acc = 0; int idx = 0;
        for (int f = 0; f < DF; f++) { float2 t = tw[idx]; acc += z[f].x * t.x - z[f].y * t.y; idx += d; if (idx >= D) idx -= D; }
        out[s * (2 * D) + D + d] = f2bf(acc * (1.f / (L * D)));
    }
}
static void naive_launch(void* const* d_in, void* d_out, void* d_ws, hipStream_t stream) {
    float* K = (float*)((char*)d_ws + (64 << 20));
    float2* X1 = (float2*)((char*)d_ws + (80 << 20));
    n_kgen<<<D, 256, 0, stream>>>((const u16*)d_in[1], (const u16*)d_in[2], (const u16*)d_in[3], (const u16*)d_in[4], (const u16*)d_in[5], (const u16*)d_in[6], (const u16*)d_in[7], K);
    n_conv<<<D, 512, 0, stream>>>((const u16*)d_in[0], K, (const u16*)d_in[8], (u16*)d_out);
    n_rowdft<<<L, 320, 0, stream>>>((const u16*)d_in[9], X1);
    n_col<<<DF, 1024, 0, stream>>>(X1, (const u16*)d_in[10], (const u16*)d_in[11]);
    n_rowidft<<<L, 576, 0, stream>>>(X1, (u16*)d_out);
}

extern "C" void kernel_launch(void* const* d_in, const int* in_sizes, int n_in,
                              void* d_out, int out_size, void* d_ws, size_t ws_size,
                              hipStream_t stream) {
    naive_launch(d_in, d_out, d_ws, stream);
}
```

```cpp
#include <hip/hip_runtime.h>
#include <hip/hip_bf16.h>

typedef unsigned short u16;
typedef unsigned long long u64;
typedef float f2 __attribute__((ext_vector_type(2)));
typedef float f4 __attribute__((ext_vector_type(4)));
typedef float f16v __attribute__((ext_vector_type(16)));
typedef __bf16 bf2 __attribute__((ext_vector_type(2)));
typedef __bf16 bf8 __attribute__((ext_vector_type(8)));
typedef unsigned u2 __attribute__((ext_vector_type(2)));
typedef unsigned u3 __attribute__((ext_vector_type(3)));
typedef unsigned u4 __attribute__((ext_vector_type(4)));
typedef __amdgpu_buffer_rsrc_t rsrc_t;
#define DI __device__ __forceinline__
#define LAS __attribute__((address_space(3)))
typedef LAS char* lds_t;

#define DLY1 6
#define DLY2 6

constexpr int NTHR = 768;
constexpr int LDS_BYTES = 152 * 1024;
constexpr int AUX_SC1 = 16;
constexpr unsigned SPIN_CAP = 1u << 17;
constexpr int WS_FLAGS = 4096;
constexpr int WS_E = 1 << 20;
constexpr int SLOT_ROW = 145 * 128;
#define STAMP(i) do { } while (0)

struct Params {
    const u16 *u, *are, *aim, *bre, *bim, *cre, *cim, *logdt, *dskip, *x, *wr, *wi;
    u16* out;
    char* ws;
};

DI unsigned pk(float a, float b) { f2 v = {a, b}; return __builtin_bit_cast(unsigned, __builtin_convertvector(v, bf2)); }
DI unsigned pk(f2 v) { return __builtin_bit_cast(unsigned, __builtin_convertvector(v, bf2)); }
DI float lo2f(unsigned v) { return __uint_as_float(v << 16); }
DI float hi2f(unsigned v) { return __uint_as_float(v & 0xffff0000u); }
DI f2 unpk(unsigned v) { return (f2){lo2f(v), hi2f(v)}; }
DI f2 fma2(f2 a, f2 b, f2 c) { return __builtin_elementwise_fma(a, b, c); }
DI f2 fma2(float a, f2 b, f2 c) { return __builtin_elementwise_fma((f2){a, a}, b, c); }
DI f4 mfma(u4 a, u4 b, f4 c) { return __builtin_amdgcn_mfma_f32_16x16x32_bf16(__builtin_bit_cast(bf8, a), __builtin_bit_cast(bf8, b), c, 0, 0, 0); }
DI f4 mfma0(u4 a, u4 b) { return mfma(a, b, (f4){0.f, 0.f, 0.f, 0.f}); }
DI u4 pk4(f4 re, f4 im) { return (u4){pk(re.x, im.x), pk(re.y, im.y), pk(re.z, im.z), pk(re.w, im.w)}; }
DI float cosr(float rev) { return __builtin_amdgcn_cosf(rev); }
DI float sinr(float rev) { return __builtin_amdgcn_sinf(rev); }
DI rsrc_t mkrsrc(const void* p, unsigned bytes) { return __builtin_amdgcn_make_buffer_rsrc((void*)p, 0, bytes, 0x00020000); }
template <class T> DI T ldsr(lds_t b, int off) { return *(LAS T*)(b + off); }
template <class T> DI void ldsw(lds_t b, int off, T v) { *(LAS T*)(b + off) = v; }
DI void barrier() { asm volatile("s_waitcnt lgkmcnt(0)\n\ts_barrier" ::: "memory"); }
DI void cfrag(const float (&rev)[4], float sc, u4& fre, u4& fim) {
#pragma unroll
    for (int i = 0; i < 4; i++) { float c = cosr(rev[i]) * sc, s = sinr(rev[i]) * sc; fre[i] = pk(c, -s); fim[i] = pk(s, c); }
}

constexpr float C91 = 0.76604444311897803520f, C92 = 0.17364817766693034885f, C94 = -0.93969262078590838405f;
constexpr float S91 = 0.64278760968653932632f, S92 = 0.98480775301220805937f, S93 = 0.86602540378443864676f, S94 = 0.34202014332566873304f;
DI void dft9_even(f2 x0, f2 t1, f2 t2, f2 t3, f2 t4, f2 (&e)[5]) {
    f2 p = t1 + t2 + t4, q = x0 + t3, r = fma2(-0.5f, t3, x0);
    e[0] = q + p; e[3] = fma2(-0.5f, p, q);
    e[1] = fma2(C94, t4, fma2(C92, t2, fma2(C91, t1, r)));
    e[2] = fma2(C91, t4, fma2(C94, t2, fma2(C92, t1, r)));
    e[4] = fma2(C92, t4, fma2(C91, t2, fma2(C94, t1, r)));
}
DI void dft9_odd(f2 d1, f2 d2, f2 d3, f2 d4, f2 (&o)[5]) {
    f2 e = S93 * d3;
    o[3] = S93 * (d1 - d2 + d4);
    o[1] = fma2(S94, d4, fma2(S92, d2, fma2(S91, d1, e)));
    o[2] = fma2(-S91, d4, fma2(S94, d2, fma2(S92, d1, -e)));
    o[4] = fma2(-S92, d4, fma2(-S91, d2, fma2(S94, d1, e)));
}

constexpr int B_TT = 0;
constexpr int B_M1 = 18432;
constexpr int B_M3 = B_M1 + 16384;
constexpr int B_X2 = B_M3 + 16384;
constexpr int B_T3 = B_X2 + 8 * 2112;

DI void role_b(const Params& p, lds_t sm, const int s2, const int c) {
    const int lane = threadIdx.x & 63, w = __builtin_amdgcn_readfirstlane(threadIdx.x >> 6);
    const int col = lane & 15, g = lane >> 4;
    const bool rowwg = s2 >= 0;
    const rsrc_t rE = mkrsrc(p.ws + WS_E, 128 * SLOT_ROW);
    if (w > 8) return;
    if (w == 8) {
        const u64 ep = *(const u64*)p.ws + 1;
        const rsrc_t rF = mkrsrc(p.ws + WS_FLAGS + ((unsigned)ep & 7u) * 8192, 8192);
        if (rowwg) {
            const rsrc_t rC = mkrsrc(p.ws + WS_E + s2 * SLOT_ROW, SLOT_ROW);
#pragma unroll
            for (int i = 0; i < 18; i++) __builtin_amdgcn_raw_buffer_store_b128((u4){~0u, ~0u, ~0u, ~0u}, rC, lane * 16, i * 1024, AUX_SC1);
            if (lane < 8) __builtin_amdgcn_raw_buffer_store_b128((u4){~0u, ~0u, ~0u, ~0u}, rC, lane * 16, 18 * 1024, AUX_SC1);
            barrier();
            asm volatile("s_waitcnt vmcnt(0)" ::: "memory");
            if (lane < 8) __builtin_amdgcn_raw_buffer_store_b64((u2){(unsigned)ep, (unsigned)(ep >> 32)}, rF, s2 * 64 + lane * 8, 0, AUX_SC1);
            STAMP(1);
            barrier();
        } else {
            __builtin_amdgcn_s_sleep(40);
        }
        for (unsigned spin = 0; spin < SPIN_CAP; spin++) {
            asm volatile("" ::: "memory");
            u2 f0 = __builtin_amdgcn_raw_buffer_load_b64(rF, lane * 64, 0, AUX_SC1);
            u2 f1 = __builtin_amdgcn_raw_buffer_load_b64(rF, lane * 64, 4096, AUX_SC1);
            bool ok = f0.x == (unsigned)ep && f0.y == (unsigned)(ep >> 32) && f1.x == (unsigned)ep && f1.y == (unsigned)(ep >> 32);
            if (__all(ok)) break;
            __builtin_amdgcn_s_sleep(2);
        }
        STAMP(2);
        barrier();
        if (s2 == 0) {
            barrier(); barrier(); barrier();
            if (lane == 0) __hip_atomic_store((u64*)p.ws, ep, __ATOMIC_RELAXED, __HIP_MEMORY_SCOPE_AGENT);
        }
        return;
    }

    const int s1r = 2 * w + (lane >> 5), ap = lane & 31;
    f2 twc[4], tws[4];
    u2 v1[2][4];
    u4 v1s = {0, 0, 0, 0};
    const int pvo = g * 4608 + col * 8;
    if (rowwg) {
        const rsrc_t rx = mkrsrc(p.x, 2048 * 1152);
        unsigned xin[9];
#pragma unroll
        for (int j = 0; j < 9; j++) xin[j] = __builtin_amdgcn_raw_buffer_load_b32(rx, (128 * s1r + s2) * 1152 + ap * 4, j * 128, 0);
        {
            const int kind = w >> 2, h = (w >> 1) & 1, ks = w & 1;
            const float m = (float)(2 * (16 * h + col) + 1 - kind) * (kind ? -1.f / 128 : 1.f / 128);
            const float a0 = (float)(32 * ks + 8 * g);
            u4 fc, fs;
#pragma unroll
            for (int j = 0; j < 4; j++) {
                float r0 = (a0 + 2 * j) * m, r1 = (a0 + 2 * j + 1) * m;
                fc[j] = pk(cosr(r0), cosr(r1)); fs[j] = pk(sinr(r0), sinr(r1));
                if (kind && h == 0 && col == 0) fs[j] = 0xBF803F80u;
            }
            ldsw(sm, B_M1 + w * 2048 + lane * 16, fc); ldsw(sm, B_M1 + w * 2048 + 1024 + lane * 16, fs);
        }
#pragma unroll
        for (int m = 0; m < 4; m++) {
            float r0 = (float)(2 * ap) * ((7 - 2 * m) * (1.f / 1152)), r1 = (float)(2 * ap + 1) * ((7 - 2 * m) * (1.f / 1152));
            twc[m] = (f2){cosr(r0), cosr(r1)}; tws[m] = (f2){sinr(r0), sinr(r1)};
        }
        u4 gre, gim;
        { float rev[4];
#pragma unroll
          for (int i = 0; i < 4; i++) rev[i] = (float)((128 * (4 * g + i) + s2) * col) * (-1.f / 2048);
          cfrag(rev, 1.f, gre, gim); }
        barrier();
        u4 m1[2][2][2];
        if (w < 5) {
#pragma unroll
            for (int i = 0; i < 8; i++) m1[i >> 2][(i >> 1) & 1][i & 1] = ldsr<u4>(sm, B_M1 + (w >> 2) * 8192 + i * 1024 + lane * 16);
        }
        STAMP(1);
        {
            f2 x[9];
#pragma unroll
            for (int j = 0; j < 9; j++) x[j] = unpk(xin[j]);
            STAMP(2);
            f2 e[5], o[5];
            dft9_even(x[0], x[1] + x[8], x[2] + x[7], x[3] + x[6], x[4] + x[5], e);
            dft9_odd(x[1] - x[8], x[2] - x[7], x[3] - x[6], x[4] - x[5], o);
            const int wa = B_TT + s1r * 128 + (((ap >> 2) ^ w) << 4) + (ap & 3) * 4;
            ldsw(sm, wa, pk(e[0]));
#pragma unroll
            for (int m = 1; m < 5; m++) {
                ldsw(sm, wa + (2 * m - 1) * 2048, pk(fma2(o[m], tws[m - 1], e[m] * twc[m - 1])));
                ldsw(sm, wa + (2 * m) * 2048, pk(fma2(-o[m], twc[m - 1], e[m] * tws[m - 1])));
            }
        }
        barrier();
        STAMP(3);
        const int ra0 = B_TT + col * 128 + ((g ^ (col >> 1)) << 4), ra1 = ra0 ^ 64;
        if (w < 4) {
            const int pb = (2 * w + 1) * 2048;
            u4 ar0 = ldsr<u4>(sm, ra0 + pb), ar1 = ldsr<u4>(sm, ra1 + pb), ai0 = ldsr<u4>(sm, ra0 + pb + 2048), ai1 = ldsr<u4>(sm, ra1 + pb + 2048);
#pragma unroll
            for (int h = 0; h < 2; h++) {
                f4 cr = mfma(ar1, m1[h][1][0], mfma0(ar0, m1[h][0][0])), sr = mfma(ar1, m1[h][1][1], mfma0(ar0, m1[h][0][1]));
                f4 ci = mfma(ai1, m1[h][1][0], mfma0(ai0, m1[h][0][0])), si = mfma(ai1, m1[h][1][1], mfma0(ai0, m1[h][0][1]));
                u4 yp = pk4(cr + si, ci - sr), ym = pk4(cr - si, -ci - sr);
                u4 zp = pk4(mfma0(gre, yp), mfma0(gim, yp)), zm = pk4(mfma0(gre, ym), mfma0(gim, ym));
#pragma unroll
                for (int i = 0; i < 4; i++) {
                    v1[h][i] = (u2){zp[i], zm[i]};
                    __builtin_amdgcn_raw_buffer_store_b64(v1[h][i], rE, pvo + i * 1152, s2 * SLOT_ROW + (1 + 2 * w + h) * 128, AUX_SC1);
                }
            }
        } else if (w == 4) {
            u4 a0 = ldsr<u4>(sm, ra0), a1 = ldsr<u4>(sm, ra1);
            u4 y0 = pk4(mfma(a1, m1[0][1][0], mfma0(a0, m1[0][0][0])), mfma(a1, m1[0][1][1], mfma0(a0, m1[0][0][1])));
            u4 y1 = pk4(mfma(a1, m1[1][1][0], mfma0(a0, m1[1][0][0])), mfma(a1, m1[1][1][1], mfma0(a0, m1[1][0][1])));
            u4 ys = y0 >> 16;
            y0 &= col == 0 ? 0xffffu : ~0u;
            u4 z0 = pk4(mfma0(gre, y0), mfma0(gim, y0)), z1 = pk4(mfma0(gre, y1), mfma0(gim, y1));
            v1s = pk4(mfma0(gre, ys), mfma0(gim, ys));
#pragma unroll
            for (int i = 0; i < 4; i++) {
                v1[0][i] = (u2){z0[i], z1[i]};
                __builtin_amdgcn_raw_buffer_store_b64(v1[0][i], rE, pvo + i * 1152, s2 * SLOT_ROW, AUX_SC1);
            }
            if (col == 0) __builtin_amdgcn_raw_buffer_store_b128(v1s, rE, g * 16, s2 * SLOT_ROW + 144 * 128, AUX_SC1);
        }
        STAMP(4);
    }
    {
        const bool spec = c == 144;
        const int k1 = c / 9, pi = c - 9 * k1;
        const int colm = spec ? (col & 7) : col;
        float wre[2][4], wim[2][4];
        {
            const rsrc_t rwr = mkrsrc(p.wr, 2048 * 289 * 2), rwi = mkrsrc(p.wi, 2048 * 289 * 2);
            const int fb = (pi + 1) >> 1, h = (pi + 1) & 1;
#pragma unroll
            for (int t = 0; t < 2; t++) {
                int f = pi == 0 ? 9 * (16 * t + col) : 9 * (16 * h + col) + (t ? 9 - fb : fb), kk = k1;
                if (spec) { f = 288; kk = 2 * colm + t; }
                const int vo = ((kk + 32 * w + 512 * g) * 289 + f) * 2;
#pragma unroll
                for (int i = 0; i < 4; i++) {
                    const int so = (16 * (i & 1) + 256 * (i >> 1)) * 578;
                    wre[t][i] = lo2f(__builtin_amdgcn_raw_buffer_load_b16(rwr, vo, so, 0));
                    wim[t][i] = lo2f(__builtin_amdgcn_raw_buffer_load_b16(rwi, vo, so, 0));
                }
            }
        }
        u4 f1r, f1i, f2r, f2i, f3r, f3i, f4r, f4i;
        { float r1[4], r4[4];
#pragma unroll
          for (int i = 0; i < 4; i++) { r1[i] = (float)((8 * (4 * g + i) + w) * col) * (-1.f / 128); r4[i] = (float)((8 * col + w) * (4 * g + i)) * (1.f / 128); }
          cfrag(r1, 1.f, f1r, f1i); cfrag(r4, 1.f, f4r, f4i);
#pragma unroll
          for (int b = 0; b < 2; b++) {
              float rv = (float)((2 * g + b) * (col >> 1)) * 0.125f, cc = cosr(rv), ss = sinr(rv);
              unsigned a = pk(cc, ss), bq = pk(-ss, cc), cq = pk(cc, -ss), d = pk(ss, cc);
              bool e1 = col & 1;
              f2r[2 * b] = e1 ? 0 : a; f2r[2 * b + 1] = e1 ? a : 0; f2i[2 * b] = e1 ? 0 : bq; f2i[2 * b + 1] = e1 ? bq : 0;
              f3r[2 * b] = e1 ? 0 : cq; f3r[2 * b + 1] = e1 ? cq : 0; f3i[2 * b] = e1 ? 0 : d; f3i[2 * b + 1] = e1 ? d : 0;
          } }
        if (rowwg) {
            const int kind = w >> 2, a = 16 * (w & 3) + col;
            const float sc = 2.f / (2048.f * 576.f);
#pragma unroll
            for (int h = 0; h < 2; h++) {
                u4 fr;
#pragma unroll
                for (int i = 0; i < 4; i++) {
                    float rv = (float)(a * (2 * (16 * h + 4 * g + i) + 1 - kind)) * (1.f / 128);
                    float cc = cosr(rv) * sc, ss = sinr(rv) * -sc;
                    if (h == 0 && i == 0 && kind && g == 0) { cc = 0.5f * sc; ss = (a & 1) ? -0.5f * sc : 0.5f * sc; }
                    fr[i] = pk(cc, ss);
                }
                ldsw(sm, B_M3 + w * 2048 + h * 1024 + lane * 16, fr);
            }
            __builtin_amdgcn_s_sleep(DLY1);
        }
        barrier();
        STAMP(5);
        const int evo = g * (32 * SLOT_ROW) + colm * 8, eso = w * SLOT_ROW + c * 128;
        u2 vin[4];
        for (unsigned spin = 0; spin < SPIN_CAP; spin++) {
            asm volatile("" ::: "memory");
#pragma unroll
            for (int i = 0; i < 4; i++) vin[i] = __builtin_amdgcn_raw_buffer_load_b64(rE, evo, eso + i * (8 * SLOT_ROW), AUX_SC1);
            unsigned mx = max(max(max(vin[0].x, vin[0].y), max(vin[1].x, vin[1].y)), max(max(vin[2].x, vin[2].y), max(vin[3].x, vin[3].y)));
            if (!__any(mx == ~0u)) break;
        }
        STAMP(6);
        const int xw = B_X2 + w * 2112 + lane * 8;
        const int xr = B_X2 + g * 4224 + ((w & 1) * 64 + (w >> 1) * 16 + col) * 8;
#pragma unroll
        for (int t = 0; t < 2; t++) {
            u4 b = {vin[0][t], vin[1][t], vin[2][t], vin[3][t]};
            u4 uu = pk4(mfma0(f1r, b), mfma0(f1i, b));
            ldsw(sm, xw + t * 1024, (u2){uu.x, uu.y}); ldsw(sm, xw + t * 1024 + 512, (u2){uu.z, uu.w});
        }
        barrier();
#pragma unroll
        for (int t = 0; t < 2; t++) {
            u2 b0 = ldsr<u2>(sm, xr + t * 1024), b1 = ldsr<u2>(sm, xr + t * 1024 + 2112);
            u4 b = {b0.x, b0.y, b1.x, b1.y};
            f4 xre = mfma0(f2r, b), xim = mfma0(f2i, b);
            f4 wr4 = {wre[t][0], wre[t][1], wre[t][2], wre[t][3]}, wi4 = {wim[t][0], wim[t][1], wim[t][2], wim[t][3]};
            f4 yre = __builtin_elementwise_fma(-xim, wi4, xre * wr4), yim = __builtin_elementwise_fma(xim, wr4, xre * wi4);
            u4 y = pk4(yre, yim);
            u4 uu = pk4(mfma0(f3r, y), mfma0(f3i, y));
            ldsw(sm, xr + t * 1024, (u2){uu.x, uu.y}); ldsw(sm, xr + t * 1024 + 2112, (u2){uu.z, uu.w});
        }
        barrier();
        u4 z[2];
#pragma unroll
        for (int t = 0; t < 2; t++) {
            u2 b0 = ldsr<u2>(sm, xw + t * 1024), b1 = ldsr<u2>(sm, xw + t * 1024 + 512);
            u4 b = {b0.x, b0.y, b1.x, b1.y};
            z[t] = pk4(mfma0(f4r, b), mfma0(f4i, b));
        }
#pragma unroll
        for (int i = 0; i < 4; i++)
            __builtin_amdgcn_raw_buffer_store_b64((u2){~(vin[i].x ^ z[0][i]), ~(vin[i].y ^ z[1][i])}, rE, evo, eso + i * (8 * SLOT_ROW), AUX_SC1);
        STAMP(7);
    }
    if (!rowwg) return;
    if (w < 5) {
        u4 gre, gim;
        { float rev[4];
#pragma unroll
          for (int i = 0; i < 4; i++) rev[i] = (float)((128 * col + s2) * (4 * g + i)) * (1.f / 2048);
          cfrag(rev, 1.f, gre, gim); }
        u4 m3[4][2];
#pragma unroll
        for (int i = 0; i < 8; i++) m3[i >> 1][i & 1] = ldsr<u4>(sm, B_M3 + (w >> 2) * 8192 + i * 1024 + lane * 16);
        const int ta = B_T3 + col * 256;
        __builtin_amdgcn_s_sleep(DLY2);
        STAMP(8);
        if (w < 4) {
            u4 z[2][2];
            for (unsigned spin = 0; spin < SPIN_CAP; spin++) {
                asm volatile("" ::: "memory");
                u2 r[2][4];
#pragma unroll
                for (int h = 0; h < 2; h++)
#pragma unroll
                    for (int i = 0; i < 4; i++) r[h][i] = __builtin_amdgcn_raw_buffer_load_b64(rE, pvo + i * 1152, s2 * SLOT_ROW + (1 + 2 * w + h) * 128, AUX_SC1);
                unsigned mx = 0;
#pragma unroll
                for (int h = 0; h < 2; h++)
#pragma unroll
                    for (int i = 0; i < 4; i++) { z[h][0][i] = ~(r[h][i].x ^ v1[h][i].x); z[h][1][i] = ~(r[h][i].y ^ v1[h][i].y); mx = max(mx, max(z[h][0][i], z[h][1][i])); }
                if (!__any(mx == ~0u)) break;
            }
            STAMP(9);
            u4 br[2], bi[2];
#pragma unroll
            for (int h = 0; h < 2; h++) {
                f4 pr = mfma0(z[h][0], gre), pi = mfma0(z[h][0], gim), mr = mfma0(z[h][1], gre), mi = mfma0(z[h][1], gim);
                br[h] = pk4(pr + mr, pi + mi); bi[h] = pk4(pi - mi, mr - pr);
            }
#pragma unroll
            for (int at = 0; at < 4; at++) {
                const int o = ta + (((4 * at + g) ^ (col & 7)) << 4) + (2 * w + 1) * 4096;
                ldsw(sm, o, mfma(m3[at][1], br[1], mfma0(m3[at][0], br[0])));
                ldsw(sm, o + 4096, mfma(m3[at][1], bi[1], mfma0(m3[at][0], bi[0])));
            }
        } else {
            u4 z0, z1, zs;
            for (unsigned spin = 0; spin < SPIN_CAP; spin++) {
                asm volatile("" ::: "memory");
                u2 r[4];
#pragma unroll
                for (int i = 0; i < 4; i++) r[i] = __builtin_amdgcn_raw_buffer_load_b64(rE, pvo + i * 1152, s2 * SLOT_ROW, AUX_SC1);
                u4 rs = __builtin_amdgcn_raw_buffer_load_b128(rE, g * 16, s2 * SLOT_ROW + 144 * 128, AUX_SC1);
                unsigned mx = 0;
#pragma unroll
                for (int i = 0; i < 4; i++) {
                    z0[i] = ~(r[i].x ^ v1[0][i].x); z1[i] = ~(r[i].y ^ v1[0][i].y); zs[i] = col == 0 ? ~(rs[i] ^ v1s[i]) : 0u;
                    mx = max(max(mx, zs[i]), max(z0[i], z1[i]));
                }
                if (!__any(mx == ~0u)) break;
            }
            STAMP(9);
            f4 r0 = mfma0(z0, gre), i0 = mfma0(z0, gim), r1 = mfma0(z1, gre), i1 = mfma0(z1, gim), rs = mfma0(zs, gre);
            if (g == 0) i0.x = rs.x;
            u4 b0 = pk4(r0, i0), b1 = pk4(r1, i1);
#pragma unroll
            for (int at = 0; at < 4; at++)
                ldsw(sm, ta + (((4 * at + g) ^ (col & 7)) << 4), mfma(m3[at][1], b1, mfma0(m3[at][0], b0)));
        }
    }
    barrier();
    STAMP(10);
    {
        const int ra = B_T3 + s1r * 256 + (((ap >> 1) ^ (s1r & 7)) << 4) + (ap & 1) * 8;
        f2 v0 = ldsr<f2>(sm, ra), vr[5], vi[5];
#pragma unroll
        for (int m = 1; m < 5; m++) {
            f2 tr = ldsr<f2>(sm, ra + (2 * m - 1) * 4096), ti = ldsr<f2>(sm, ra + (2 * m) * 4096);
            vr[m] = fma2(ti, tws[m - 1], tr * twc[m - 1]);
            vi[m] = fma2(-tr, tws[m - 1], ti * twc[m - 1]);
        }
        f2 e[5], o[5];
        dft9_even(v0, vr[1], vr[2], vr[3], vr[4], e);
        dft9_odd(vi[1], vi[2], vi[3], vi[4], o);
        const rsrc_t ro = mkrsrc(p.out, 2048 * 2304);
        const int vo = (128 * s1r + s2) * 2304 + 1152 + ap * 4;
        __builtin_amdgcn_raw_buffer_store_b32(pk(e[0]), ro, vo, 0, AUX_SC1);
#pragma unroll
        for (int j = 1; j < 5; j++) {
            __builtin_amdgcn_raw_buffer_store_b32(pk(e[j] - o[j]), ro, vo, j * 128, AUX_SC1);
            __builtin_amdgcn_raw_buffer_store_b32(pk(e[j] + o[j]), ro, vo, (9 - j) * 128, AUX_SC1);
        }
    }
    STAMP(11);
}

constexpr int A_U = 0;
constexpr int A_UST = 4608;
constexpr int A_KD = 6 * A_UST;
constexpr int A_CNT = A_KD + 6 * 3072;
constexpr int A_ST = A_CNT + 64;
constexpr int A_TB = A_ST;
constexpr int A_TBST = 5696;
constexpr int A_T2 = 4864, A_T3 = 4864 + 512;
constexpr int A_Y = A_TB + 6 * A_TBST;
static_assert(A_ST + 12 * 8704 <= LDS_BYTES, "lds");

DI f2 cmul(f2 a, f2 b, f2 bs) { return fma2(a.yy, bs, a.xx * b); }
DI f2 rot(f2 b) { return (f2){-b.y, b.x}; }
DI f2 cpow(float a2, float rv, float e) {
    float m = __builtin_amdgcn_exp2f(a2 * e), t = __builtin_amdgcn_fractf(rv * e);
    return (f2){m * cosr(t), m * sinr(t)};
}
DI int kd_addr(int l, int zero) {
    int v = l <= 256 ? l : ((l + ((l >> 1) & 1)) & ~1);
    v = v > 512 ? 512 : v;
    int i = v <= 256 ? v - 1 : v - 258;
    int row = v <= 256 ? (i & 7) : 8 + ((i & 7) >> 1);
    int a = row * 128 + (i >> 3) * 4;
    a = l == 0 ? 20 * 128 : a;
    return (l < 0 || l > 514) ? zero : a;
}
DI int near_addr(int l) { return ((l + 16) & 7) * 608 + ((l + 16) >> 3) * 8; }

template <int LO, int HI>
DI void conv_part(const Params& p, lds_t sm, int slot, int nch, int cw, float dsk) {
    const int lane = threadIdx.x & 63, col = lane & 15, g = lane >> 4;
    const int tb = A_TB + slot * A_TBST;
    u4 an[17];
    {
        const int a0 = tb + near_addr(16 + col - 8 * g), a1 = tb + near_addr(12 + col - 8 * g);
#pragma unroll
        for (int j = 0; j < 17; j++) { u2 x = ldsr<u2>(sm, a0 + 32 * j), y = ldsr<u2>(sm, a1 + 32 * j); an[j] = (u4){x.x, x.y, y.x, y.y}; }
    }
    u4 bd3, bd5, bd7, bd9, aex;
    {
        const unsigned k1024 = pk(ldsr<float>(sm, A_KD + slot * 3072 + 15 * 128 + 31 * 4), 0.f);
#pragma unroll
        for (int j = 0; j < 4; j++) {
            const int d0 = 8 * g + 2 * j - col - 8, d1 = d0 + 1;
            auto in = [](int d, int lo, int hi) { return d >= lo && d <= hi; };
            bd3[j] = (in(d0, 3, 5) ? 0x3F80u : 0u) | (in(d1, 3, 5) ? 0x3F800000u : 0u);
            bd5[j] = (in(d0, -2, 2) ? 0x3F80u : 0u) | (in(d1, -2, 2) ? 0x3F800000u : 0u);
            bd7[j] = (in(d0, -3, 3) ? 0x3F80u : 0u) | (in(d1, -3, 3) ? 0x3F800000u : 0u);
            bd9[j] = (in(d0, -4, 4) ? 0x3F80u : 0u) | (in(d1, -4, 4) ? 0x3F800000u : 0u);
            aex[j] = (in(d0, 4, 5) ? k1024 : 0u) | (in(d1, 4, 5) ? k1024 << 16 : 0u);
        }
    }
    const int ub = A_U + slot * A_UST + 512 - 32 + col * 32 + g * 16;
    const int t2 = tb + A_T2 + (col - 4 * g - 17) * 8, t3 = tb + A_T3 + (col - 4 * g - 48) * 4;
    const rsrc_t ro = mkrsrc(p.out, 2048 * 2304);
    f4 acc[8];
#pragma unroll
    for (int n = 0; n < 8; n++) acc[n] = (f4){0.f, 0.f, 0.f, 0.f};
#define IN(n) ((n) >= LO && (n) <= HI)
#pragma unroll
    for (int nu = 0; nu <= HI; nu++) {
        const u4 w0 = ldsr<u4>(sm, ub + 512 * nu);
        if (IN(nu) || IN(nu + 1)) {
#pragma unroll
            for (int r = 0; r < 8; r++) {
                const u4 f = r == 0 ? w0 : ldsr<u4>(sm, ub + 512 * nu - 64 * r);
                if (IN(nu)) acc[nu] = mfma(an[r], f, acc[nu]);
                if (IN(nu + 1)) acc[(nu + 1) & 7] = mfma(an[r + 8], f, acc[(nu + 1) & 7]);
            }
        }
        if (IN(nu + 2)) acc[(nu + 2) & 7] = mfma(an[16], w0, acc[(nu + 2) & 7]);
        if (IN(nu)) {
            const int to = (256 * nu + 16 * col + 4 * g) * 2;
            const u2 uu = ldsr<u2>(sm, A_U + slot * A_UST + 512 + to);
            const unsigned z0 = pk(unpk(uu.x) * dsk), z1 = pk(unpk(uu.y) * dsk);
            const unsigned y0 = pk(acc[nu].x, acc[nu].y), y1 = pk(acc[nu].z, acc[nu].w);
            ldsw(sm, A_Y + slot * 4096 + to, (u2){pk(unpk(y0) + unpk(z0)), pk(unpk(y1) + unpk(z1))});
            unsigned arrived = 0;
            asm volatile("" ::: "memory");
            if (lane == 0) arrived = __hip_atomic_fetch_add((LAS unsigned*)(sm + A_CNT + nu * 4), 1u, __ATOMIC_RELAXED, __HIP_MEMORY_SCOPE_WORKGROUP);
            asm volatile("" ::: "memory");
            if (__builtin_amdgcn_readfirstlane(arrived) == (unsigned)(nch - 1)) {
                u2 y[6];
#pragma unroll
                for (int s = 0; s < 6; s++) y[s] = ldsr<u2>(sm, A_Y + s * 4096 + 512 * nu + lane * 8);
                const int vo = (256 * nu + 4 * lane) * 2304 + cw * 2;
#pragma unroll
                for (int i = 0; i < 4; i++) {
                    const unsigned sel = (i & 1) ? 0x07060302u : 0x05040100u;
                    const unsigned d0 = __builtin_amdgcn_perm(y[1][i >> 1], y[0][i >> 1], sel), d1 = __builtin_amdgcn_perm(y[3][i >> 1], y[2][i >> 1], sel),
                                   d2 = __builtin_amdgcn_perm(y[5][i >> 1], y[4][i >> 1], sel);
                    if (nch == 6) __builtin_amdgcn_raw_buffer_store_b96((u3){d0, d1, d2}, ro, vo, i * 2304, 0);
                    else __builtin_amdgcn_raw_buffer_store_b64((u2){d1, d2}, ro, vo + 4, i * 2304, 0);
                }
            }
        }
        const bool s2on = IN(nu + 2) || IN(nu + 3) || IN(nu + 4), s3on = IN(nu + 4) || IN(nu + 5) || IN(nu + 6) || IN(nu + 7);
        if (s2on || s3on) {
            const u4 w1 = ldsr<u4>(sm, ub + 512 * nu - 16);
            if (s2on) {
                const f4 b4 = mfma0(w0, bd3), b8 = mfma0(w0, bd5), b12 = mfma0(w1, bd3), b16 = mfma0(w1, bd5);
                const u4 q0 = {pk(b4.x, b8.x), pk(b12.x, b16.x), pk(b4.y, b8.y), pk(b12.y, b16.y)};
                const u4 q1 = {pk(b4.z, b8.z), pk(b12.z, b16.z), pk(b4.w, b8.w), pk(b12.w, b16.w)};
#pragma unroll
                for (int d = 2; d <= 4; d++) if (IN(nu + d)) {
                    const u2 k0 = ldsr<u2>(sm, t2 + 128 * d), k1 = ldsr<u2>(sm, t2 + 128 * d - 8), k2 = ldsr<u2>(sm, t2 + 128 * d - 16), k3 = ldsr<u2>(sm, t2 + 128 * d - 24);
                    acc[(nu + d) & 7] = mfma(q1, (u4){k2.x, k2.y, k3.x, k3.y}, mfma(q0, (u4){k0.x, k0.y, k1.x, k1.y}, acc[(nu + d) & 7]));
                }
            }
            if (s3on) {
                const u4 q = pk4(mfma0(w0, bd7), mfma0(w1, bd9));
#pragma unroll
                for (int d = 4; d <= 7; d++) if (IN(nu + d)) {
                    const u4 k = {ldsr<unsigned>(sm, t3 + 64 * d), ldsr<unsigned>(sm, t3 + 64 * d - 4), ldsr<unsigned>(sm, t3 + 64 * d - 8), ldsr<unsigned>(sm, t3 + 64 * d - 12)};
                    acc[(nu + d) & 7] = mfma(q, k, acc[(nu + d) & 7]);
                }
            }
            if (IN(nu + 4)) acc[(nu + 4) & 7] = mfma(aex, w0, acc[(nu + 4) & 7]);
        }
    }
#undef IN
}

DI void role_a(const Params& p, lds_t sm, const int grp) {
    const int lane = threadIdx.x & 63, w = __builtin_amdgcn_readfirstlane(threadIdx.x >> 6);
    const int tri = grp / 3, r = grp - 3 * tri;
    const int cw = 16 * tri + (r == 2 ? 10 : 6 * r), nch = r == 2 ? 4 : 6;
    if (w >= 2 * nch) return;
    const int slot = (w >> 1) + (r == 2 ? 2 : 0), half = w & 1, ch = cw + slot;
    const int po = (ch * 128 + half * 64 + lane) * 2;
    const unsigned rare = __builtin_amdgcn_raw_buffer_load_b16(mkrsrc(p.are, 576 * 256), po, 0, 0), raim = __builtin_amdgcn_raw_buffer_load_b16(mkrsrc(p.aim, 576 * 256), po, 0, 0);
    const unsigned rbre = __builtin_amdgcn_raw_buffer_load_b16(mkrsrc(p.bre, 576 * 256), po, 0, 0), rbim = __builtin_amdgcn_raw_buffer_load_b16(mkrsrc(p.bim, 576 * 256), po, 0, 0);
    const unsigned rcre = __builtin_amdgcn_raw_buffer_load_b16(mkrsrc(p.cre, 576 * 256), po, 0, 0), rcim = __builtin_amdgcn_raw_buffer_load_b16(mkrsrc(p.cim, 576 * 256), po, 0, 0);
    const unsigned rldt = __builtin_amdgcn_raw_buffer_load_b16(mkrsrc(p.logdt, 1152), ch * 2, 0, 0), rdsk = __builtin_amdgcn_raw_buffer_load_b16(mkrsrc(p.dskip, 1152), ch * 2, 0, 0);
    barrier();
    u3 uin[4];
    if (w < 8) {
        const rsrc_t ru = mkrsrc(p.u, 2048 * 1152);
#pragma unroll
        for (int i = 0; i < 4; i++) uin[i] = __builtin_amdgcn_raw_buffer_load_b96(ru, (256 * w + 4 * lane) * 1152 + cw * 2, i * 1152, 0);
    }
    if (half == 0) ldsw(sm, A_U + slot * A_UST + lane * 8, (u2){0u, 0u});
    ldsw(sm, A_KD + slot * 3072 + half * 1536 + lane * 16, (u4){0u, 0u, 0u, 0u});
    ldsw(sm, A_KD + slot * 3072 + half * 1536 + 1024 + lane * 8, (u2){0u, 0u});
    if (w == 0 && lane < 16) ldsw(sm, A_CNT + lane * 4, 0u);
    const int kd = A_KD + slot * 3072, zero = A_CNT + 32 - kd;
    int ga[7], xa[4];
#pragma unroll
    for (int i = 0; i < 7; i++) ga[i] = kd + kd_addr(8 * lane + 4 * half - 2 + i, zero);
    const int xl = lane < 13 ? lane - 12 : 500 + lane;
#pragma unroll
    for (int i = 0; i < 4; i++) xa[i] = kd + kd_addr(xl - i, zero);
    barrier();
    STAMP(1);
    const float dt = lo2f(pk(expf(lo2f(rldt)), 0.f));
    const float a2 = dt * lo2f(rare) * 1.44269504088896340736f, rv = dt * lo2f(raim) * 0.15915494309189533577f;
    const int st = A_ST + w * 8704;
    const int sw = st + lane * 4;
    STAMP(2);
    f2 z8;
    {
        const f2 z1 = cpow(a2, rv, 1.f), z256 = cpow(a2, rv, 256.f), r1 = rot(z1), r256 = rot(z256);
        z8 = cpow(a2, rv, 8.f);
        const f2 z2 = cmul(z1, z1, r1), z3 = cmul(z2, z1, r1), z4 = cmul(z3, z1, r1), z5 = cmul(z4, z1, r1), z6 = cmul(z5, z1, r1), z7 = cmul(z6, z1, r1);
        const f2 z258 = cmul(z2, z256, r256), z260 = cmul(z4, z256, r256), z262 = cmul(z6, z256, r256), z264 = cmul(z8, z256, r256);
        const f2 z516 = cmul(z260, z256, r256), z520 = cmul(z264, z256, r256), z772 = cmul(z516, z256, r256), z776 = cmul(z520, z256, r256);
        const f2 z1032 = cmul(z776, z256, r256), z1288 = cmul(z1032, z256, r256), z1544 = cmul(z1288, z256, r256), z1800 = cmul(z1544, z256, r256);
        const f2 pr[20] = {z1, z2, z3, z4, z5, z6, z7, z8, z258, z260, z262, z264, z516, z520, z772, z776, z1032, z1288, z1544, z1800};
#pragma unroll
        for (int i = 0; i < 20; i++) ldsw(sm, sw + i * 272, pk(pr[i]));
        ldsw(sm, sw + 20 * 272, 0x3F80u);
    }
    const int sr = st + (lane & 31) * 272 + (lane >> 5) * 16;
    u4 pa[8];
    asm volatile("" ::: "memory");
#pragma unroll
    for (int ks = 0; ks < 8; ks++) pa[ks] = ldsr<u4>(sm, sr + 32 * ks);
    asm volatile("" ::: "memory");
    {
        const f2 b = {dt * lo2f(rbre), dt * lo2f(rbim)}, cc = {lo2f(rcre), lo2f(rcim)};
        const f2 c = cmul(cc, b, rot(b));
        const f2 y8 = {z8.x, -z8.y}, ry8 = rot(y8);
        f2 q0 = {c.x, -c.y}, q1 = cmul(q0, y8, ry8);
        const f2 y16 = cmul(y8, y8, ry8), ry16 = rot(y16);
#pragma unroll
        for (int q = 0; q < 32; q += 2) {
            ldsw(sm, sw + q * 272, pk(q0)); ldsw(sm, sw + (q + 1) * 272, pk(q1));
            if (q < 30) { q0 = cmul(q0, y16, ry16); q1 = cmul(q1, y16, ry16); }
        }
    }
    asm volatile("" ::: "memory");
    {
        f16v kacc;
#pragma unroll
        for (int i = 0; i < 16; i++) kacc[i] = 0.f;
#pragma unroll
        for (int ks = 0; ks < 8; ks++)
            kacc = __builtin_amdgcn_mfma_f32_32x32x16_bf16(__builtin_bit_cast(bf8, pa[ks]), __builtin_bit_cast(bf8, ldsr<u4>(sm, sr + 32 * ks)), kacc, 0, 0, 0);
        const int ka = kd + (lane >> 5) * 512 + (lane & 31) * 4;
#pragma unroll
        for (int i = 0; i < 12; i++)
            __hip_atomic_fetch_add((LAS float*)(sm + ka + ((i & 3) + 8 * (i >> 2)) * 128), kacc[i], __ATOMIC_RELAXED, __HIP_MEMORY_SCOPE_WORKGROUP);
    }
    STAMP(3);
    barrier();
    STAMP(4);
    {
        const int tb = A_TB + slot * A_TBST;
        float x[7];
#pragma unroll
        for (int i = 0; i < 7; i++) x[i] = ldsr<float>(sm, ga[i]);
        unsigned d[6];
#pragma unroll
        for (int i = 0; i < 6; i++) d[i] = pk(x[i + 1], x[i]);
#pragma unroll
        for (int i = 0; i < 4; i++) ldsw(sm, tb + near_addr(8 * lane + 4 * half + 1 + i), (u2){d[i + 2], d[i]});
        if (half == 0) {
            const u2 e = {pk(ldsr<float>(sm, xa[0]), ldsr<float>(sm, xa[1])), pk(ldsr<float>(sm, xa[2]), ldsr<float>(sm, xa[3]))};
            if (lane < 44) ldsw(sm, tb + near_addr(xl), e);
            if (lane < 40) ldsw(sm, tb + A_T2 + (lane < 15 ? lane : lane + 32) * 8, (u2){0u, 0u});
        } else {
            const int ra = kd + (12 + 2 * (lane >> 5)) * 128 + (lane & 31) * 4;
            ldsw(sm, tb + A_T2 + 120 + lane * 4, pk(ldsr<float>(sm, ra), ldsr<float>(sm, ra + 128)));
            ldsw(sm, tb + A_T3 + 64 + lane * 4, pk(ldsr<f2>(sm, kd + 16 * 128 + lane * 8)));
        }
    }
    STAMP(5);
    if (w < 8) {
#pragma unroll
        for (int d = 0; d < 3; d++) {
            const int o = A_U + 2 * d * A_UST + 512 + 512 * w + lane * 8;
            ldsw(sm, o, (u2){__builtin_amdgcn_perm(uin[1][d], uin[0][d], 0x05040100u), __builtin_amdgcn_perm(uin[3][d], uin[2][d], 0x05040100u)});
            ldsw(sm, o + A_UST, (u2){__builtin_amdgcn_perm(uin[1][d], uin[0][d], 0x07060302u), __builtin_amdgcn_perm(uin[3][d], uin[2][d], 0x07060302u)});
        }
    }
    STAMP(6);
    barrier();
    STAMP(7);
    if (half == 0) conv_part<0, 4>(p, sm, slot, nch, cw, lo2f(rdsk));
    else conv_part<5, 7>(p, sm, slot, nch, cw, lo2f(rdsk));
    STAMP(11);
}

__global__ void __launch_bounds__(NTHR, 3) fused_kernel(Params p) {
    __shared__ __attribute__((aligned(16))) char smem[LDS_BYTES];
    lds_t sm = (lds_t)smem;
    STAMP(0);
    const int x = blockIdx.x & 7, q = blockIdx.x >> 3;
    const bool spec = blockIdx.x == 255;
    if (q < 18 || spec) {
        role_b(p, sm, q < 16 ? x * 16 + q : -1, spec ? 144 : x * 18 + q);
    } else if (q < 30 || x < 6) {
        role_a(p, sm, q < 30 ? x * 12 + q - 18 : 96 + x * 2 + q - 30);
    }
}

extern "C" void kernel_launch(void* const* d_in, const int* in_sizes, int n_in, void* d_out, int out_size, void* d_ws, size_t ws_size,
                              hipStream_t stream) {
    Params p;
    p.u = (const u16*)d_in[0]; p.are = (const u16*)d_in[1]; p.aim = (const u16*)d_in[2]; p.bre = (const u16*)d_in[3]; p.bim = (const u16*)d_in[4];
    p.cre = (const u16*)d_in[5]; p.cim = (const u16*)d_in[6]; p.logdt = (const u16*)d_in[7]; p.dskip = (const u16*)d_in[8];
    p.x = (const u16*)d_in[9]; p.wr = (const u16*)d_in[10]; p.wi = (const u16*)d_in[11];
    p.out = (u16*)d_out; p.ws = (char*)d_ws;
    fused_kernel<<<256, NTHR, 0, stream>>>(p);
}
```

```cpp
#include <hip/hip_runtime.h>
#include <hip/hip_bf16.h>

typedef unsigned short u16;
typedef unsigned long long u64;
typedef float f2 __attribute__((ext_vector_type(2)));
typedef float f4 __attribute__((ext_vector_type(4)));
typedef __bf16 bf16x2 __attribute__((ext_vector_type(2)));
typedef __bf16 bf16x8 __attribute__((ext_vector_type(8)));
typedef unsigned u2 __attribute__((ext_vector_type(2)));
typedef unsigned u3 __attribute__((ext_vector_type(3)));
typedef unsigned u4 __attribute__((ext_vector_type(4)));
typedef short s4v __attribute__((ext_vector_type(4)));
typedef unsigned u8v __attribute__((ext_vector_type(8)));
typedef unsigned u16v __attribute__((ext_vector_type(16)));
typedef __amdgpu_buffer_rsrc_t rsrc_t;
#define DI __device__ __forceinline__
#define LAS __attribute__((address_space(3)))
#define GAS __attribute__((address_space(1)))
typedef LAS char* lds_t;

constexpr int DLY1 = 14;
constexpr int DLY2 = 17;
constexpr int DLYF = 8;
constexpr int DLYW = 10;
constexpr int DLYP = 7;
constexpr int WGD = 28;
constexpr int FPS = 2;
constexpr int CD = 4;
constexpr int UPD0 = 4;
constexpr int DLYU = 20;
constexpr int IOS = 2;
constexpr int IOK = 2;
constexpr int IOT = 5;
constexpr int QT = 6;
constexpr int SW = 4;
constexpr int NPF = 5;
constexpr int EP = 2;
constexpr int RT1 = 2;
constexpr int RT2 = 4;
constexpr int BAUX = 2;

constexpr int NTHR = 1024;
constexpr int LDS_BYTES = 158 * 1024;
constexpr int AUX_SC1 = 16;
constexpr unsigned SPIN_CAP = 1u << 19;
constexpr int WS_FLAGS = 4096;
constexpr int WS_E = 1 << 20;
constexpr int SLOT_ROW = 147 * 128;
#define FINISH asm volatile("s_endpgm" ::: "memory")

struct Params {
    const u16 *u, *are, *aim, *bre, *bim, *cre, *cim, *logdt, *dskip, *x, *wr, *wi;
    u16* out;
    char* ws;
};
enum { ROLE_B1, ROLE_B2, ROLE_AC, ROLE_IO, ROLE_NONE };
struct Tab { unsigned d[256][2]; };

DI unsigned pk(float a, float b) { f2 v = {a, b}; return __builtin_bit_cast(unsigned, __builtin_convertvector(v, bf16x2)); }
DI unsigned pk(f2 v) { return __builtin_bit_cast(unsigned, __builtin_convertvector(v, bf16x2)); }
DI float lo2f(unsigned v) { return __uint_as_float(v << 16); }
DI float hi2f(unsigned v) { return __uint_as_float(v & 0xffff0000u); }
DI f2 unpk(unsigned v) { return (f2){lo2f(v), hi2f(v)}; }
DI f2 fma2(f2 a, f2 b, f2 c) { return __builtin_elementwise_fma(a, b, c); }
DI f2 fma2(float a, f2 b, f2 c) { return __builtin_elementwise_fma((f2){a, a}, b, c); }
DI f4 mfma(u4 a, u4 b, f4 c) { return __builtin_amdgcn_mfma_f32_16x16x32_bf16(__builtin_bit_cast(bf16x8, a), __builtin_bit_cast(bf16x8, b), c, 0, 0, 0); }
DI f4 mfma0(u4 a, u4 b) { return mfma(a, b, (f4){0.f, 0.f, 0.f, 0.f}); }
DI void mfma_acc(f4& c, u4 a, u4 b) { asm volatile("v_mfma_f32_16x16x32_bf16 %0, %1, %2, %0" : "+v"(c) : "v"(a), "v"(b)); }
DI void mfma_new(f4& c, u4 a, u4 b) { asm volatile("v_mfma_f32_16x16x32_bf16 %0, %1, %2, 0" : "=&v"(c) : "v"(a), "v"(b)); }
DI u4 pk4(f4 re, f4 im) { return (u4){pk(re.x, im.x), pk(re.y, im.y), pk(re.z, im.z), pk(re.w, im.w)}; }
DI float cosr(float rev) { return __builtin_amdgcn_cosf(rev); }
DI float sinr(float rev) { return __builtin_amdgcn_sinf(rev); }
DI rsrc_t mkrsrc(const void* p, unsigned bytes) { return __builtin_amdgcn_make_buffer_rsrc((void*)p, 0, bytes, 0x00020000); }
template <class T> DI T gload(const void* p, unsigned off, int imm = 0) { return *(const GAS T*)((const GAS char*)p + off + imm); }
template <class T> DI T ldsr(lds_t b, int off) { return *(LAS T*)(b + off); }
template <class T> DI void ldsw(lds_t b, int off, T v) { *(LAS T*)(b + off) = v; }
DI unsigned lds_wait1(lds_t sm, int off, unsigned want) {
    unsigned v = 0;
#pragma unroll 1
    for (unsigned spin = 0; spin < (SPIN_CAP << 3); spin++) {
        asm volatile("" ::: "memory");
        v = __builtin_amdgcn_readfirstlane(ldsr<unsigned>(sm, off));
        if (v >= want) break;
        __builtin_amdgcn_s_sleep(1);
    }
    asm volatile("" ::: "memory");
    return v;
}
template <int OFF> DI void lds_count(lds_t sm) {
    asm volatile("s_mov_b64 exec, 1\n\tds_add_u32 %0, %1 offset:%2\n\ts_mov_b64 exec, -1" :: "v"((unsigned)(size_t)sm), "v"(1u), "i"(OFF) : "memory");
}
constexpr int L_CNT = 0;
DI void barrier() { asm volatile("s_waitcnt lgkmcnt(0)\n\ts_barrier" ::: "memory"); }
#define CODE_MARK(n) asm volatile(".Lfk_m" #n ":")
DI int lane_id() { int l; asm volatile("v_mbcnt_lo_u32_b32 %0, -1, 0\n\tv_mbcnt_hi_u32_b32 %0, -1, %0" : "=v"(l)); __builtin_assume(l >= 0 && l < 64); return l; }
template <int V> struct IC { static constexpr int v = V; };
template <int I, int N, class F> DI void static_for(F&& f) { if constexpr (I < N) { f(IC<I>{}); static_for<I + 1, N>(f); } }
template <class T> DI void pin(T& v) { asm volatile("" : "+v"(v)); }
template <class T, int N> DI void kill(T (&v)[N]) {
#pragma unroll
    for (int i = 0; i < N; i++) asm volatile("" : "=v"(v[i]));
}
DI f2 lo2(f4 v) { return __builtin_shufflevector(v, v, 0, 1); }
DI f2 hi2(f4 v) { return __builtin_shufflevector(v, v, 2, 3); }
DI u4 pk4(f2 r0, f2 r1, f2 i0, f2 i1) { return (u4){pk(r0.x, i0.x), pk(r0.y, i0.y), pk(r1.x, i1.x), pk(r1.y, i1.y)}; }

constexpr float C91 = 0.76604444311897803520f, C92 = 0.17364817766693034885f, C94 = -0.93969262078590838405f;
constexpr float S91 = 0.64278760968653932632f, S92 = 0.98480775301220805937f, S93 = 0.86602540378443864676f, S94 = 0.34202014332566873304f;
DI void dft9_even(f2 x0, f2 t1, f2 t2, f2 t3, f2 t4, f2 (&e)[5]) {
    const f2 p = t1 + t2 + t4, q = x0 + t3, r = fma2(-0.5f, t3, x0), a = t1 - t4, b = t2 - t4;
    e[0] = q + p; e[3] = fma2(-0.5f, p, q);
    e[1] = fma2(C92, b, fma2(C91, a, r));
    e[2] = fma2(C94, b, fma2(C92, a, r));
    e[4] = fma2(C91, b, fma2(C94, a, r));
}
DI void dft9_odd(f2 d1, f2 d2, f2 d3, f2 d4, f2 (&o)[5]) {
    const f2 e = S93 * d3, a = d1 + d2, b = d2 + d4;
    o[3] = S93 * (d1 - d2 + d4);
    o[1] = fma2(S94, b, fma2(S91, a, e));
    o[2] = fma2(-S91, b, fma2(S92, a, -e));
    o[4] = fma2(-S92, b, fma2(S94, a, e));
}

constexpr int B_TT = 2048;
constexpr int B_M1 = B_TT + 18432;
constexpr int B_G = B_M1 + 16384;
constexpr int B_TW = B_G + 6144;
constexpr int B_X2 = B_TW + 2048;
constexpr int B_T3 = B_X2 + 8 * 2112;

DI void b_tables(lds_t sm, int w, const int s2) {
    const int lane = threadIdx.x & 63, col = lane & 15;
    const float colf = (float)col, gf = (float)(lane >> 4);
    if (w < 8)
        {
            const int kind = w >> 2;
            const float m = fmaf(colf, 2.f, (float)(16 * (w & 2) + 1 - kind)) * (kind ? -1.f / 128 : 1.f / 128);
            const float r0 = fmaf(gf, 8.f, (float)(32 * (w & 1))) * m;
            u4 fc, fs;
#pragma unroll
            for (int j = 0; j < 4; j++) {
                const float ra = fmaf(2.f * j, m, r0), rb = fmaf(2.f * j + 1, m, r0);
                fc[j] = pk(cosr(ra), cosr(rb)); fs[j] = pk(sinr(ra), sinr(rb));
            }
            if ((w >> 1) == 2 && col == 0) fs = (u4){0xBF803F80u, 0xBF803F80u, 0xBF803F80u, 0xBF803F80u};
            ldsw(sm, B_M1 + w * 2048 + lane * 16, fc); ldsw(sm, B_M1 + w * 2048 + 1024 + lane * 16, fs);
        }
    asm volatile("" : "+s"(w));
    if (w == 8) {
#pragma unroll
        for (int m = 0; m < 4; m++) {
            const float r = (float)lane * ((7 - 2 * m) * (1.f / 1152));
            const int o = B_TW + m * 512 + (lane >> 1) * 16 + (lane & 1) * 4;
            ldsw(sm, o, cosr(r)); ldsw(sm, o + 8, sinr(r));
        }
    }
    asm volatile("" : "+s"(w));
    if (w > 8) {
        const float t = w == 11 ? 0.f : (float)s2 * (1.f / 2048), sc = w == 10 ? 2.f / (2048.f * 576.f) : 1.f;
        const float bq = w == 10 ? fmaf(colf, 1.f / 16, t) : colf * (-1.f / 16), aq = fmaf(4.f * gf, bq, w == 10 ? 0.f : -t * colf);
        u4 fre, fim;
#pragma unroll
        for (int i = 0; i < 4; i++) { const float r = fmaf((float)i, bq, aq), c = cosr(r) * sc, s = sinr(r) * sc; fre[i] = pk(c, -s); fim[i] = pk(s, c); }
        ldsw(sm, B_G + (w - 9) * 2048 + lane * 16, fre); ldsw(sm, B_G + (w - 9) * 2048 + 1024 + lane * 16, fim);
    }
    if (w < 10) lds_count<L_CNT>(sm);
}

template <int HALF> DI void read_frags(lds_t sm, int off, u4 (&f)[2]) {
    u2 a, b, c, d;
    asm volatile("ds_read_b64 %0, %4\n\tds_read_b64 %1, %4 offset:%5\n\tds_read_b64 %2, %4 offset:1024\n\tds_read_b64 %3, %4 offset:%6\n\ts_waitcnt lgkmcnt(0)"
                 : "=&v"(a), "=&v"(b), "=&v"(c), "=&v"(d) : "v"((unsigned)(size_t)sm + off), "i"(HALF), "i"(1024 + HALF) : "memory");
    f[0] = (u4){a.x, a.y, b.x, b.y}; f[1] = (u4){c.x, c.y, d.x, d.y};
}

DI void wait_flags(const Params& p, int lane, int half, u64 ep) {
    const rsrc_t rF = mkrsrc(p.ws + WS_FLAGS, 65536);
    int o = ((unsigned)ep & 7u) * 8192 + lane * 64;
#pragma unroll 1
    for (unsigned spin = 0; spin < SPIN_CAP; spin++) {
        asm volatile("" ::: "memory");
        const u2 fl = __builtin_amdgcn_raw_buffer_load_b64(rF, o, half * 4096, AUX_SC1);
        if (o < 65536 && fl.x == (unsigned)ep && fl.y == (unsigned)(ep >> 32)) o = 0x7fff0000;
        if (__all(o >= 65536)) break;
        __builtin_amdgcn_s_sleep(FPS);
    }
}

DI void role_b2(const Params& p, lds_t sm, const int w, const int s2, const int k1, const int pi, u64 ep) {
    const int lane = lane_id();
    const int col = lane & 15, g = lane >> 4;
    const float gf = (float)g;
    const bool rowwg = s2 >= 0, spec = k1 == 16;
    const rsrc_t rE = mkrsrc(p.ws + WS_E, 128 * SLOT_ROW);
    unsigned wre[2][4], wim[2][4];
    const rsrc_t rwr = mkrsrc(p.wr, 2048 * 578), rwi = mkrsrc(p.wi, 2048 * 578);
    int wv0, wv1;
    {
        const int fb = (pi + 1) >> 1;
        const int b0 = spec ? pi * (4 * 578) + 576 : k1 * 578 + (pi ? ((pi + 1) & 1) * 288 + fb * 2 : 0), dt = spec ? 578 : pi ? 18 - 4 * fb : 288;
        wv0 = g * (512 * 578) + (spec ? (col & 1) * 1156 : col * 18) + (b0 + w * (32 * 578)); wv1 = wv0 + dt;
    }
    auto wload = [&](int i) {
        const int so = (16 * (i & 1) + 256 * (i >> 1)) * 578;
        wre[0][i] = __builtin_amdgcn_raw_buffer_load_b16(rwr, wv0, so, 0); wre[1][i] = __builtin_amdgcn_raw_buffer_load_b16(rwr, wv1, so, 0);
        wim[0][i] = __builtin_amdgcn_raw_buffer_load_b16(rwi, wv0, so, 0); wim[1][i] = __builtin_amdgcn_raw_buffer_load_b16(rwi, wv1, so, 0);
    };
    unsigned pf = 0;
    if (w < 4 && !spec) {
        if (!rowwg) __builtin_amdgcn_s_sleep(4);
        pf = gload<u16>((w & 1) ? p.wi : p.wr, (k1 + 16 * min(pi + 9 * ((w & 2) * 4 + (lane >> 3)), 127)) * 578 + min((lane & 7) * 128, 576));
    }
    if (w < 2) {
        asm volatile("s_waitcnt vmcnt(1)" ::: "memory");
        if (spec) asm volatile("s_waitcnt vmcnt(0)" ::: "memory");
        pin(ep);
        ep += 1;
        ldsw(sm, L_CNT + 16, ep);
    }
    if (rowwg) {
        if (w >= 4) asm volatile("s_waitcnt vmcnt(0)" ::: "memory");
        unsigned fo = WS_FLAGS + ((unsigned)ep & 7u) * 8192 + s2 * 64 + lane * 8;
        pin(fo); pin(wv0); pin(wv1);
        barrier();
        if (w == 0 && lane < 8) __hip_atomic_store((GAS u64*)((GAS char*)p.ws + fo), ep, __ATOMIC_RELAXED, __HIP_MEMORY_SCOPE_AGENT);
    } else __builtin_amdgcn_s_sleep(WGD);
    asm volatile("" ::: "memory");
    wload(0); wload(1);
    __builtin_amdgcn_s_setprio(0);
    u4 f2r, f2i, f3r, f3i;
    {
        const float ka = (float)(2 * w + (col & 1)) * (1.f / 128), hi = (float)(col >> 1), kk = fmaf(hi, 0.125f, ka);
        const unsigned m0 = (col & 1) ? 0u : ~0u;
#pragma unroll
        for (int b = 0; b < 2; b++) {
            const float lo = fmaf(gf, 2.f, (float)b), r2 = lo * kk, r3 = hi * fmaf(lo, 0.125f, ka);
            const float c2 = cosr(r2), s2n = sinr(r2), c3 = cosr(r3), s3 = sinr(r3);
            const unsigned a = pk(c2, s2n), bb = pk(-s2n, c2), c = pk(c3, -s3), d = pk(s3, c3);
            f2r[2 * b] = a & m0; f2r[2 * b + 1] = a & ~m0; f2i[2 * b] = bb & m0; f2i[2 * b + 1] = bb & ~m0;
            f3r[2 * b] = c & m0; f3r[2 * b + 1] = c & ~m0; f3i[2 * b] = d & m0; f3i[2 * b + 1] = d & ~m0;
        }
    }
    pin(f2r); pin(f2i); pin(f3r); pin(f3i);
    int evo = g * (32 * SLOT_ROW) + (spec ? col & 7 : col) * 8;
    const int eso = w * SLOT_ROW + (spec ? 144 * 128 + pi * 64 : (k1 * 9 + pi) * 128);
    int xw = B_X2 + w * 2112 + lane * 8;
    int xr = B_X2 + g * 4224 + ((w & 1) * 64 + (w >> 1) * 16 + col) * 8;
    pin(evo); pin(xw); pin(xr);
    if (rowwg) __builtin_amdgcn_s_sleep(DLYW);
    __builtin_amdgcn_s_setprio(2);
    wload(2); wload(3);
    if (!rowwg) {
        __builtin_amdgcn_s_sleep(DLYF);
        if (w < 2) wait_flags(p, lane, w, ep);
    }
    barrier();
    u2 vin[4];
    f4 wr4[2], wi4[2];
#pragma unroll
    for (int i = 0; i < 4; i++) vin[i] = __builtin_amdgcn_raw_buffer_load_b64(rE, evo, eso + i * (8 * SLOT_ROW), AUX_SC1);
    asm volatile("" ::: "memory");
    const u4 f1r = ldsr<u4>(sm, B_G + 4096 + lane * 16), f1i = ldsr<u4>(sm, B_G + 5120 + lane * 16);
    u4 f4r = f1r ^ 0x80000000u, f4i = f1i ^ 0x8000u;
    pin(f4r); pin(f4i);
#pragma unroll
    for (int t = 0; t < 2; t++) {
#pragma unroll
        for (int i = 0; i < 4; i++) { wr4[t][i] = lo2f(wre[t][i]); wi4[t][i] = lo2f(wim[t][i]); }
        pin(wr4[t]); pin(wi4[t]);
    }
    asm volatile("" :: "v"(pf));
    auto missing = [&]() { return __any(max(max(max(vin[0].x, vin[0].y), max(vin[1].x, vin[1].y)), max(max(vin[2].x, vin[2].y), max(vin[3].x, vin[3].y))) == ~0u); };
    if (__builtin_expect(missing(), 0)) {
#pragma unroll 1
        for (unsigned spin = 0; spin < SPIN_CAP; spin++) {
            asm volatile("" ::: "memory");
#pragma unroll
            for (int i = 0; i < 4; i++) vin[i] = __builtin_amdgcn_raw_buffer_load_b64(rE, evo, eso + i * (8 * SLOT_ROW), AUX_SC1);
            if (!missing()) break;
        }
    }
    {
#pragma unroll
        for (int t = 0; t < 2; t++) {
            u4 b = {vin[0][t], vin[1][t], vin[2][t], vin[3][t]};
            u4 uu = pk4(mfma0(f1r, b), mfma0(f1i, b));
            ldsw(sm, xw + t * 1024, (u2){uu.x, uu.y}); ldsw(sm, xw + t * 1024 + 512, (u2){uu.z, uu.w});
        }
        barrier();
        u4 bb[2];
        read_frags<2112>(sm, xr, bb);
        __builtin_amdgcn_sched_barrier(0);
        const f4 xr0 = mfma0(f2r, bb[0]), xi0 = mfma0(f2i, bb[0]), xr1 = mfma0(f2r, bb[1]), xi1 = mfma0(f2i, bb[1]);
        __builtin_amdgcn_sched_barrier(0);
#pragma unroll
        for (int t = 0; t < 2; t++) {
            const f4 xre = t ? xr1 : xr0, xim = t ? xi1 : xi0;
            u4 y = pk4(fma2(-lo2(xim), lo2(wi4[t]), lo2(xre) * lo2(wr4[t])), fma2(-hi2(xim), hi2(wi4[t]), hi2(xre) * hi2(wr4[t])),
                       fma2(lo2(xim), lo2(wr4[t]), lo2(xre) * lo2(wi4[t])), fma2(hi2(xim), hi2(wr4[t]), hi2(xre) * hi2(wi4[t])));
            u4 uu = pk4(mfma0(f3r, y), mfma0(f3i, y));
            ldsw(sm, xr + t * 1024, (u2){uu.x, uu.y}); ldsw(sm, xr + t * 1024 + 2112, (u2){uu.z, uu.w});
        }
        barrier();
        u4 z[2];
        read_frags<512>(sm, xw, bb);
#pragma unroll
        for (int t = 0; t < 2; t++) z[t] = pk4(mfma0(f4r, bb[t]), mfma0(f4i, bb[t]));
        CODE_MARK(4);
#pragma unroll
        for (int i = 0; i < 4; i++)
            __builtin_amdgcn_raw_buffer_store_b64((u2){~(vin[i].x ^ z[0][i]), ~(vin[i].y ^ z[1][i])}, rE, evo, eso + i * (8 * SLOT_ROW), AUX_SC1);
    }
}

DI void role_b1(const Params& p, lds_t sm, const int w, const int s2, const unsigned (&xin)[9]) {
    const int lane = threadIdx.x & 63;
    const int col = lane & 15, g = lane >> 4;
    const rsrc_t rE = mkrsrc(p.ws + WS_E, 128 * SLOT_ROW);
    __builtin_amdgcn_s_setprio(2);
    const int s1r = 2 * w + (lane >> 5), ap = lane & 31;
    u2 v1[2][4];
    const int pvo = g * 4608 + col * 8;
    const int pso = s2 * SLOT_ROW + (w < 4 ? 1 + 2 * w : 0) * 128;
    f4 tw[4];
    {
        lds_wait1(sm, L_CNT, 10);
        u4 m1[2][2][2], gre, gim;
#pragma unroll
        for (int m = 0; m < 4; m++) tw[m] = ldsr<f4>(sm, B_TW + m * 512 + ap * 16);
        if (w < 5) {
#pragma unroll
            for (int i = 0; i < 8; i++) m1[i >> 2][(i >> 1) & 1][i & 1] = ldsr<u4>(sm, B_M1 + (w >> 2) * 8192 + i * 1024 + lane * 16);
            gre = ldsr<u4>(sm, B_G + lane * 16); gim = ldsr<u4>(sm, B_G + 1024 + lane * 16);
        }
        int wa = B_TT + s1r * 128 + (((ap >> 2) ^ w) << 4) + (ap & 3) * 4;
        int ra0 = B_TT + col * 128 + ((g ^ (col >> 1)) << 4) + (w < 4 ? (2 * w + 1) * 2048 : 0), ra1 = ra0 ^ 64;
        pin(wa); pin(ra0); pin(ra1);
        {
            f2 x[9];
#pragma unroll
            for (int j = 0; j < 9; j++) x[j] = unpk(xin[j]);
            f2 e[5], o[5], tr[5], ti[5];
            dft9_even(x[0], x[1] + x[8], x[2] + x[7], x[3] + x[6], x[4] + x[5], e);
            dft9_odd(x[1] - x[8], x[2] - x[7], x[3] - x[6], x[4] - x[5], o);
#pragma unroll
            for (int m = 1; m < 5; m++) { tr[m] = e[m] * lo2(tw[m - 1]); ti[m] = e[m] * hi2(tw[m - 1]); }
#pragma unroll
            for (int m = 1; m < 5; m++) { tr[m] = fma2(o[m], hi2(tw[m - 1]), tr[m]); ti[m] = fma2(-o[m], lo2(tw[m - 1]), ti[m]); }
            __builtin_amdgcn_sched_barrier(0);
            ldsw(sm, wa, pk(e[0]));
#pragma unroll
            for (int m = 1; m < 5; m++) { ldsw(sm, wa + (2 * m - 1) * 2048, pk(tr[m])); ldsw(sm, wa + (2 * m) * 2048, pk(ti[m])); }
        }
        barrier();
        if (w < 4) {
            u4 ar0 = ldsr<u4>(sm, ra0), ai0 = ldsr<u4>(sm, ra0 + 2048), ar1 = ldsr<u4>(sm, ra1), ai1 = ldsr<u4>(sm, ra1 + 2048);
#pragma unroll
            for (int h = 0; h < 2; h++) {
                f4 cr = mfma(ar1, m1[h][1][0], mfma0(ar0, m1[h][0][0])), sr = mfma(ar1, m1[h][1][1], mfma0(ar0, m1[h][0][1]));
                f4 ci = mfma(ai1, m1[h][1][0], mfma0(ai0, m1[h][0][0])), si = mfma(ai1, m1[h][1][1], mfma0(ai0, m1[h][0][1]));
                u4 yp = pk4(lo2(cr) + lo2(si), hi2(cr) + hi2(si), lo2(ci) - lo2(sr), hi2(ci) - hi2(sr));
                u4 ym = pk4(lo2(cr) - lo2(si), hi2(cr) - hi2(si), -lo2(ci) - lo2(sr), -hi2(ci) - hi2(sr));
                u4 zp = pk4(mfma0(gre, yp), mfma0(gim, yp)), zm = pk4(mfma0(gre, ym), mfma0(gim, ym));
#pragma unroll
                for (int i = 0; i < 4; i++) {
                    v1[h][i] = (u2){zp[i], zm[i]};
                    __builtin_amdgcn_raw_buffer_store_b64(v1[h][i], rE, pvo + i * 1152 + h * 128, pso, AUX_SC1);
                }
            }
        } else if (w == 4) {
            u4 a0 = ldsr<u4>(sm, ra0), a1 = ldsr<u4>(sm, ra1);
            u4 y0 = pk4(mfma(a1, m1[0][1][0], mfma0(a0, m1[0][0][0])), mfma(a1, m1[0][1][1], mfma0(a0, m1[0][0][1])));
            u4 y1 = pk4(mfma(a1, m1[1][1][0], mfma0(a0, m1[1][0][0])), mfma(a1, m1[1][1][1], mfma0(a0, m1[1][0][1])));
            u4 ys = y0 >> 16;
            y0 &= col == 0 ? 0xffffu : ~0u;
            u4 z0 = pk4(mfma0(gre, y0), mfma0(gim, y0)), z1 = pk4(mfma0(gre, y1), mfma0(gim, y1));
            u4 zs = pk4(mfma0(gre, ys), mfma0(gim, ys));
#pragma unroll
            for (int i = 0; i < 4; i++) {
                v1[0][i] = (u2){z0[i], z1[i]};
                __builtin_amdgcn_raw_buffer_store_b64(v1[0][i], rE, pvo + i * 1152, pso, AUX_SC1);
            }
            v1[1][0] = (u2){zs.x, zs.y}; v1[1][1] = (u2){zs.z, zs.w};
            if (col < 4) __builtin_amdgcn_raw_buffer_store_b128(zs, rE, g * 64 + col * 16, pso + 144 * 128, AUX_SC1);
        } else if (w < 7) {
            const u64 ep = ldsr<u64>(sm, L_CNT + 16);
            __builtin_amdgcn_s_sleep(DLY1);
            wait_flags(p, lane, w - 5, ep);
        }
        if (w < 5) __builtin_amdgcn_s_sleep(DLYP);
    }
    __builtin_amdgcn_s_setprio(0);
    barrier();
    int ra = B_T3 + s1r * 256 + (((ap >> 1) ^ (s1r & 7)) << 4) + (ap & 1) * 8;
    int ovo = (128 * s1r + s2) * 2304 + 1152 + ap * 4;
    pin(ra); pin(ovo);
    if (w >= 5) { barrier(); barrier(); __builtin_amdgcn_s_setprio(2); }
    else {
        u4 gre = ldsr<u4>(sm, B_G + 2048 + lane * 16), gim = ldsr<u4>(sm, B_G + 3072 + lane * 16);
        u4 m3[4][2];
        const int mt = B_M1 + (w >> 2) * 8192 + ((lane & 2) * 8 + (lane >> 2)) * 16 + (lane & 1) * 8;
#pragma unroll
        for (int i = 0; i < 8; i++) {
            const int o = mt + (i >> 2) * 2048 + (i & 1) * 1024 + (i & 2) * 256;
            const u2 h0 = __builtin_bit_cast(u2, __builtin_amdgcn_ds_read_tr16_b64_v4i16((LAS s4v*)(sm + o))), h1 = __builtin_bit_cast(u2, __builtin_amdgcn_ds_read_tr16_b64_v4i16((LAS s4v*)(sm + o + 4096)));
            m3[i >> 1][i & 1] = (u4){h0.x, h0.y, h1.x, h1.y};
            pin(m3[i >> 1][i & 1]);
        }
        int ta0 = B_T3 + col * 256 + ((g ^ (col & 7)) << 4) + (w < 4 ? (2 * w + 1) * 4096 : 0), ta1 = ta0 ^ 64;
        pin(ta0); pin(ta1); pin(gre); pin(gim);
        barrier(); barrier();
        asm volatile("s_waitcnt vmcnt(0)" ::: "memory");
        __builtin_amdgcn_s_sleep(DLY2);
        __builtin_amdgcn_s_setprio(2);
        if (w < 4) {
            u4 z[2][2];
#pragma unroll 1
            for (unsigned spin = 0; spin < SPIN_CAP; spin++) {
                asm volatile("" ::: "memory");
                u2 r[2][4];
#pragma unroll
                for (int h = 0; h < 2; h++)
#pragma unroll
                    for (int i = 0; i < 4; i++) r[h][i] = __builtin_amdgcn_raw_buffer_load_b64(rE, pvo + i * 1152 + h * 128, pso, AUX_SC1);
                unsigned mx = 0;
#pragma unroll
                for (int h = 0; h < 2; h++)
#pragma unroll
                    for (int i = 0; i < 4; i++) { z[h][0][i] = ~(r[h][i].x ^ v1[h][i].x); z[h][1][i] = ~(r[h][i].y ^ v1[h][i].y); mx = max(mx, max(z[h][0][i], z[h][1][i])); }
                if (!__any(mx == ~0u)) break;
            }
            u4 br[2], bi[2];
#pragma unroll
            for (int h = 0; h < 2; h++) {
                f4 pr = mfma0(z[h][0], gre), pi = mfma0(z[h][0], gim), mr = mfma0(z[h][1], gre), mi = mfma0(z[h][1], gim);
                br[0][2 * h] = pk(lo2(pr) + lo2(mr)); br[0][2 * h + 1] = pk(hi2(pr) + hi2(mr)); br[1][2 * h] = pk(-lo2(pi) - lo2(mi)); br[1][2 * h + 1] = pk(-hi2(pi) - hi2(mi));
                bi[0][2 * h] = pk(lo2(pi) - lo2(mi)); bi[0][2 * h + 1] = pk(hi2(pi) - hi2(mi)); bi[1][2 * h] = pk(lo2(pr) - lo2(mr)); bi[1][2 * h + 1] = pk(hi2(pr) - hi2(mr));
            }
#pragma unroll
            for (int at = 0; at < 4; at++) {
                const int o = ((at & 1) ? ta1 : ta0) + (at >> 1) * 128;
                ldsw(sm, o, mfma(m3[at][1], br[1], mfma0(m3[at][0], br[0])));
                ldsw(sm, o + 4096, mfma(m3[at][1], bi[1], mfma0(m3[at][0], bi[0])));
            }
        } else {
            u4 z0, z1, zs;
#pragma unroll 1
            for (unsigned spin = 0; spin < SPIN_CAP; spin++) {
                asm volatile("" ::: "memory");
                u2 r[4];
#pragma unroll
                for (int i = 0; i < 4; i++) r[i] = __builtin_amdgcn_raw_buffer_load_b64(rE, pvo + i * 1152, pso, AUX_SC1);
                u4 rs = __builtin_amdgcn_raw_buffer_load_b128(rE, g * 64, pso + 144 * 128, AUX_SC1);
                unsigned mx = 0;
#pragma unroll
                for (int i = 0; i < 4; i++) {
                    z0[i] = ~(r[i].x ^ v1[0][i].x); z1[i] = ~(r[i].y ^ v1[0][i].y); zs[i] = col == 0 ? ~(rs[i] ^ v1[1][i >> 1][i & 1]) : 0u;
                    mx = max(max(mx, zs[i]), max(z0[i], z1[i]));
                }
                if (!__any(mx == ~0u)) break;
            }
            f4 r0 = mfma0(z0, gre), i0 = mfma0(z0, gim), r1 = mfma0(z1, gre), i1 = mfma0(z1, gim), rs = mfma0(zs, gre);
            if (g == 0) { r0.x *= 0.5f; i0.x = 0.5f * rs.x; }
            u4 b0 = {pk(lo2(r0)), pk(hi2(r0)), pk(lo2(r1)), pk(hi2(r1))}, b1 = {pk(lo2(i0)), pk(hi2(i0)), pk(lo2(i1)), pk(hi2(i1))};
#pragma unroll
            for (int at = 0; at < 4; at++)
                ldsw(sm, ((at & 1) ? ta1 : ta0) + (at >> 1) * 128, mfma(m3[at][1], b1, mfma0(m3[at][0], b0)));
        }
    }
    barrier();
    {
        f2 v0 = ldsr<f2>(sm, ra), vr[5], vi[5];
#pragma unroll
        for (int m = 1; m < 5; m++) {
            f2 tr = ldsr<f2>(sm, ra + (2 * m - 1) * 4096), ti = ldsr<f2>(sm, ra + (2 * m) * 4096);
            vr[m] = fma2(ti, hi2(tw[m - 1]), tr * lo2(tw[m - 1]));
            vi[m] = fma2(-tr, hi2(tw[m - 1]), ti * lo2(tw[m - 1]));
        }
        f2 e[5], o[5];
        dft9_even(v0, vr[1], vr[2], vr[3], vr[4], e);
        dft9_odd(vi[1], vi[2], vi[3], vi[4], o);
        const rsrc_t ro = mkrsrc(p.out, 2048 * 2304);
        CODE_MARK(5);
        __builtin_amdgcn_raw_buffer_store_b32(pk(e[0]), ro, ovo, 0, BAUX);
#pragma unroll
        for (int j = 1; j < 5; j++) {
            __builtin_amdgcn_raw_buffer_store_b32(pk(e[j] - o[j]), ro, ovo + j * 128, 0, BAUX);
            __builtin_amdgcn_raw_buffer_store_b32(pk(e[j] + o[j]), ro, ovo + (9 - j) * 128, 0, BAUX);
        }
    }
    if (s2 == 0 && threadIdx.x == 0) __hip_atomic_store((GAS u64*)p.ws, ldsr<u64>(sm, L_CNT + 16), __ATOMIC_RELAXED, __HIP_MEMORY_SCOPE_AGENT);
}

constexpr int A_CNT = L_CNT + 64;
constexpr int A_ST = 256;
constexpr int A_STB = 4608, RB = 6 * A_STB;
constexpr int A_U = A_ST + 12 * A_STB;
constexpr int A_UST = 4608;
constexpr int A_NEAR = A_U + 6 * A_UST;
constexpr int A_Y = A_NEAR + 6 * 4864;
constexpr int R_T2 = 0, R_T3 = 512;
static_assert(A_Y + 6 * 8192 <= LDS_BYTES, "lds");

DI f2 cmul(f2 a, f2 b, f2 bs) { return fma2(a.yy, bs, a.xx * b); }
DI f2 rot(f2 b) { return (f2){-b.y, b.x}; }
DI f2 cpow(f2 ar) { float m = __builtin_amdgcn_exp2f(ar.x), t = __builtin_amdgcn_fractf(ar.y); return (f2){cosr(t), sinr(t)} * m; }
template <int ROW> DI void wrow(unsigned v) { asm volatile("ds_write_addtid_b32 %0 offset:%1" :: "v"(v), "i"(ROW * 288) : "memory"); }
template <int Q, int END> DI void qrows(f2& q0, f2& q1, f2 y, f2 ry) {
    if constexpr (Q < END) {
        __builtin_amdgcn_sched_barrier(0);
        f2 t0, t1;
        if (Q < 30) { t0 = q0.xx * y; t1 = q1.xx * y; }
        const unsigned c0 = pk(q0), c1 = pk(q1);
        __builtin_amdgcn_sched_barrier(0);
        if (Q < 30) q0 = fma2(q0.yy, ry, t0);
        wrow<Q & 15>(c0);
        if (Q < 30) q1 = fma2(q1.yy, ry, t1);
        wrow<(Q + 1) & 15>(c1);
        qrows<Q + 2, END>(q0, q1, y, ry);
    }
}
struct Taps { u4 f[12]; f2 v[4]; };
DI void taps(lds_t sm, int st, int sq, const unsigned (&ld)[9], float ldt, Taps& t) {
    asm volatile("s_mov_b32 m0, %0\n\ts_nop 1" :: "s"((unsigned)(size_t)sm + st));
    const float dt = lo2f(pk(__builtin_amdgcn_exp2f(ldt * 1.44269504088896340736f), 0.f));
    const f2 ar = (f2){lo2f(ld[1]), lo2f(ld[2])} * dt * (f2){1.44269504088896340736f, 0.15915494309189533577f};
    const f2 z1 = cpow(ar), z8 = cpow(ar * 8.f), z256 = cpow(ar * 256.f), r1 = rot(z1), r256 = rot(z256);
    wrow<0>(pk(z1)); wrow<8>(pk(z256));
    const f2 z2 = cmul(z1, z1, r1), z264 = cmul(z8, z256, r256);
    wrow<1>(pk(z2));
    const f2 z3 = cmul(z2, z1, r1), z520 = cmul(z264, z256, r256), z258 = cmul(z2, z256, r256);
    wrow<2>(pk(z3)); wrow<5>(pk(z520)); wrow<9>(pk(z258));
    const f2 z4 = cmul(z3, z1, r1), z776 = cmul(z520, z256, r256);
    wrow<3>(pk(z4)); wrow<7>(pk(z776));
    const f2 z5 = cmul(z4, z1, r1), z1032 = cmul(z776, z256, r256), z260 = cmul(z4, z256, r256);
    const unsigned c260 = pk(z260);
    wrow<12>(pk(z1032)); wrow<10>(c260); wrow<11>(c260);
    const f2 z6 = cmul(z5, z1, r1), z1288 = cmul(z1032, z256, r256), z516 = cmul(z260, z256, r256);
    wrow<13>(pk(z1288)); wrow<4>(pk(z516));
    const f2 z7 = cmul(z6, z1, r1), z1544 = cmul(z1288, z256, r256), z772 = cmul(z516, z256, r256);
    wrow<14>(pk(z1544)); wrow<6>(pk(z772));
    const f2 z262 = cmul(z6, z256, r256), z1800 = cmul(z1544, z256, r256);
    wrow<15>(pk(z1800));
#pragma unroll
    for (int ks = 0; ks < 4; ks++) t.f[ks] = ldsr<u4>(sm, sq + 64 * ks);
    wrow<0>(pk(z5)); wrow<1>(pk(z6)); wrow<2>(pk(z7)); wrow<3>(pk(z8)); wrow<4>(0x3F80u); wrow<8>(c260); wrow<9>(pk(z262));
    { const unsigned c = pk(z264); wrow<10>(c); wrow<11>(c); }
#pragma unroll
    for (int ks = 0; ks < 4; ks++) t.f[4 + ks] = ldsr<u4>(sm, sq + 64 * ks);
    __builtin_amdgcn_sched_barrier(0);
    const f2 b = (f2){lo2f(ld[3]), -lo2f(ld[4])} * dt, cc = {lo2f(ld[5]), -lo2f(ld[6])};
    const f2 y8 = {z8.x, -z8.y}, ry8 = {z8.y, z8.x};
    f2 q0 = cmul(cc, b, rot(b)), q1 = cmul(q0, y8, ry8);
    const f2 y16 = cmul(y8, y8, ry8), ry16 = rot(y16);
    qrows<0, 8>(q0, q1, y16, ry16);
    __builtin_amdgcn_s_setprio(1);
    qrows<8, 16>(q0, q1, y16, ry16);
#pragma unroll
    for (int ks = 0; ks < 4; ks++) t.f[8 + ks] = ldsr<u4>(sm, sq + 64 * ks);
    qrows<16, 26>(q0, q1, y16, ry16);
    t.v[0] = q0; t.v[1] = q1; t.v[2] = y16; t.v[3] = ry16;
}
template <bool N> DI unsigned taps_end(lds_t sm, int co, int sq, int pw, Taps& t, f4 (&c)[4]) {
    const unsigned sr = N ? ldsr<unsigned>(sm, pw + 4) : ldsr<unsigned>(sm, L_CNT + 12);
    asm volatile("" ::: "memory");
#pragma unroll
    for (int i = 0; i < 4; i++) c[i] = N ? ldsr<f4>(sm, co + 1024 * i) : (f4){0.f, 0.f, 0.f, 0.f};
    qrows<26, 32>(t.v[0], t.v[1], t.v[2], t.v[3]);
    __builtin_amdgcn_s_setprio(3);
    u4 qb[4];
#pragma unroll
    for (int ks = 0; ks < 4; ks++) qb[ks] = ldsr<u4>(sm, sq + 64 * ks);
    const unsigned su = ldsr<unsigned>(sm, pw + 8);
    if (!N && __builtin_expect(__builtin_amdgcn_readfirstlane(sr) < 3, 0)) lds_wait1(sm, L_CNT + 12, 3);
    if (N && __builtin_expect(!__builtin_amdgcn_readfirstlane(sr), 0)) {
        lds_wait1(sm, pw + 4, 1);
#pragma unroll
        for (int i = 0; i < 4; i++) c[i] = ldsr<f4>(sm, co + 1024 * i);
        __builtin_amdgcn_s_waitcnt(0xC07F);
    }
    __builtin_amdgcn_sched_barrier(0);
#pragma unroll
    for (int ks = 0; ks < 4; ks++) { c[0] = mfma(t.f[ks], t.f[8 + ks], c[0]); c[1] = mfma(t.f[4 + ks], t.f[8 + ks], c[1]); }
    __builtin_amdgcn_sched_barrier(0);
#pragma unroll
    for (int ks = 0; ks < 4; ks++) { c[2] = mfma(t.f[ks], qb[ks], c[2]); c[3] = mfma(t.f[4 + ks], qb[ks], c[3]); }
    asm volatile("" ::: "memory");
    return su;
}
DI float shr1(float old, float v) { return __int_as_float(__builtin_amdgcn_update_dpp(__float_as_int(old), __float_as_int(v), 0x138, 0xf, 0xf, false)); }

template <int OFF> DI u4 rd2(unsigned a0, unsigned a1) {
    u2 x, y;
    asm volatile("ds_read_b64 %0, %2 offset:%4\n\tds_read_b64 %1, %3 offset:%4" : "=&v"(x), "=&v"(y) : "v"(a0), "v"(a1), "i"(OFF));
    return (u4){x.x, x.y, y.x, y.y};
}
template <int OFF> DI u4 rd1(unsigned a) { u4 x; asm volatile("ds_read_b128 %0, %1 offset:%2" : "=&v"(x) : "v"(a), "i"(OFF) : "memory"); return x; }
template <int N> DI void lgkm_wait() { asm volatile("s_waitcnt lgkmcnt(%0)" :: "i"(N) : "memory"); }
struct APre { unsigned m[6]; };

DI void role_an(lds_t sm, const int slot, const APre& pre, Taps& t, const float dsk) {
    const int lane = lane_id(), col = lane & 15, g = lane >> 4;
    const int pw = A_CNT + slot * 32;
    unsigned su0;
    {
        f4 c[4];
        su0 = taps_end<true>(sm, A_ST + slot * A_STB + RB + lane * 16, pre.m[5], pw, t, c);
        float ka[13];
#pragma unroll
        for (int i = 0; i < 4; i++) {
            const auto s0 = __builtin_amdgcn_permlane16_swap(__float_as_uint(c[0][i]), __float_as_uint(c[2][i]), false, false);
            const auto s1 = __builtin_amdgcn_permlane16_swap(__float_as_uint(c[1][i]), __float_as_uint(c[3][i]), false, false);
            ka[i] = __uint_as_float(s0[0]); ka[8 + i] = __uint_as_float(s0[1]); ka[4 + i] = __uint_as_float(s1[0]);
            if (i == 0) ka[12] = __uint_as_float(s1[1]) + dsk;
        }
        const unsigned c0 = pk(ka[8], ka[9]), c1 = pk(ka[10], ka[11]);
        ldsw(sm, pre.m[3], (u16)c0); ldsw(sm, pre.m[4], (u16)(c0 >> 16)); ldsw(sm, pre.m[3] + 128, (u16)c1); ldsw(sm, pre.m[4] + 128, (u16)(c1 >> 16));
        asm volatile("" ::: "memory");
        ldsw(sm, pw + 24, 1u);
        asm volatile("" ::: "memory");
        const float x[11] = {shr1(0.f, ka[5]), shr1(0.f, ka[6]), shr1(ka[12], ka[7]), ka[0], ka[1], ka[2], ka[3], ka[4], ka[5], ka[6], ka[7]};
        u2 e[8];
        const int na = pre.m[2];
#pragma unroll
        for (int i = 7; i >= 0; i--) {
            asm("v_cvt_pk_bf16_f32 %0, %1, %2" : "=v"(e[i].x) : "v"(x[i + 3]), "v"(x[i + 2]));
            asm("v_cvt_pk_bf16_f32 %0, %1, %2 ; again" : "=v"(e[i].y) : "v"(x[i + 1]), "v"(x[i]));
            asm volatile("ds_write_b64 %0, %1 offset:%2" :: "v"((unsigned)(size_t)sm + na), "v"(e[i]), "i"(i == 7 ? 8 : (i + 1) * 608) : "memory");
        }
        const unsigned d[10] = {0, e[1].y, 0, 0, 0, 0, 0, 0, e[6].x, e[7].x};
        const unsigned nq = (unsigned)(size_t)sm + na, hi = d[9] & 0xffff0000u;
        asm volatile("s_mov_b64 exec, 1\n\tds_write_b32 %0, %3\n\ts_brev_b64 exec, 1\n\t"
                     "ds_write2_b32 %0, %5, %4 offset0:154 offset1:155\n\tds_write2_b32 %1, %5, %5 offset0:2 offset1:3\n\tds_write2_b32 %1, %6, %5 offset0:154 offset1:155\n\t"
                     "ds_write2_b32 %2, %5, %6 offset0:3 offset1:155\n\ts_mov_b64 exec, -1"
                     :: "v"(nq), "v"(nq + 1216), "v"(nq + 2432), "v"(d[1]), "v"(d[8]), "v"(d[9]), "v"(hi) : "memory", "scc");
        ldsw(sm, pw + 24, 2u);
    }
    asm volatile("" ::: "memory");
    int ub = A_U + slot * A_UST + 512 - 32 + col * 32 + g * 16;
    int pa = A_Y + slot * 8192 + lane * 16, pwv = pw;
    pin(ub); pin(pa); pin(pwv);
    constexpr int PF = NPF;
    u4 an[16], fr[128];
    f4 acc[8];
    unsigned su, sr, y0, y1, pr;
    const unsigned a0 = (unsigned)(size_t)sm + pre.m[0], a1 = (unsigned)(size_t)sm + pre.m[1], ua = (unsigned)(size_t)sm + ub - 448;
    auto grp = [&](auto G) __attribute__((always_inline)) {
        an[G.v] = rd2<32 * G.v>(a0, a1); an[G.v + 8] = rd2<32 * G.v + 256>(a0, a1); fr[G.v] = rd1<448 - 64 * G.v>(ua);
    };
    if (__builtin_expect(!__builtin_amdgcn_readfirstlane(su0), 0)) lds_wait1(sm, pw + 8, 1);
    grp(IC<0>{}); grp(IC<1>{});
    constexpr int G0 = 8 * SW + 8, NF = 2 * 64 - G0;
    constexpr auto idx = [](int s) { return s < G0 ? s : 2 * s - G0; };
    constexpr auto upto = [=](int s) { const int n = s < 5 ? 8 : s < 8 ? 8 + (PF * (s - 4) + 2) / 3 : idx(s + 1) + PF + (s >= G0 - 8); return n < NF ? n : NF; };
    static_for<0, 64>([&](auto S) __attribute__((always_inline)) {
        constexpr int s = S.v, nu = s >> 3, r = s & 7;
        constexpr int pn = nu < SW - 1 ? nu + 2 : nu >= SW ? nu + 1 : 8;
        if constexpr (s < 6) grp(IC<s + 2>{});
        static_for<upto(s - 1), upto(s)>([&](auto K) __attribute__((always_inline)) {
            constexpr int k = K.v - G0;
            fr[K.v] = ldsr<u4>(sm, ub + (k < 0 ? 512 * (K.v >> 3) - 64 * (K.v & 7) : 512 * (SW + (k >> 4) + (k & 1)) - 64 * ((k & 15) >> 1)));
        });
        if constexpr (s < 8) {
            __builtin_amdgcn_sched_barrier(0);
            lgkm_wait<5 * (s < 6 ? 2 : 7 - s) + upto(s) - 8>();
        }
        if constexpr (s == 0) mfma_new(acc[0], an[0], fr[0]); else if constexpr (nu <= SW) mfma_acc(acc[nu], an[r], fr[s]); else mfma_acc(acc[nu], an[r + 8], fr[idx(s)]);
        __builtin_amdgcn_sched_barrier(0);
        if constexpr (r == 0) {
            if (nu == 0 && slot < 2) __builtin_amdgcn_s_setprio(EP);
            if (nu > 1 && ((nu & 1) || slot < 2)) __builtin_amdgcn_s_setprio(3 - (nu >> 1));
            if ((nu & 1) && nu < 7) { if (__builtin_expect(!__builtin_amdgcn_readfirstlane(su), 0)) lds_wait1(sm, pw + 10 + 2 * nu, 1); asm volatile("" ::: "memory"); }
        }
        if constexpr (r == (nu ? 0 : 2)) {
            asm volatile("" ::: "memory");
            if (pn < 8) sr = ldsr<unsigned>(sm, pwv + 4);
            if ((nu & 1) == 0 && nu < 6) su = ldsr<unsigned>(sm, pwv + 12 + 2 * nu);
        }
        if constexpr (r == (nu == SW ? 2 : 1) && nu > 0) {
            asm volatile("s_nop 1"); pin(acc[nu - 1]); y0 = pk(lo2(acc[nu - 1])); y1 = pk(hi2(acc[nu - 1])); pin(y0); pin(y1);
        }
        if constexpr (r == (nu >= QT ? 1 : 2) && nu > 0) { ldsw(sm, pa + 1024 * (nu - 1), (u2){y0, y1}); asm volatile("" ::: "memory"); pr = nu; pin(pr); }
        if constexpr (r == (nu >= QT ? 1 : 3) && nu > 0) { ldsw(sm, pwv, pr); asm volatile("" ::: "memory"); }
        if constexpr (r == (nu ? 4 : 7) && pn < 8) {
            if (__builtin_expect(__builtin_amdgcn_readfirstlane(sr) < pn, 0)) lds_wait1(sm, pw + 4, pn);
            asm volatile("" ::: "memory");
            acc[pn] = ldsr<f4>(sm, pa + 1024 * pn);
        }
        __builtin_amdgcn_sched_barrier(0);
        if constexpr (s == 0) mfma_new(acc[1], an[8], fr[0]); else if constexpr (nu < SW) mfma_acc(acc[nu + 1], an[r + 8], fr[s]); else if constexpr (nu > SW) mfma_acc(acc[nu], an[r], fr[idx(s) + 1]);
        __builtin_amdgcn_sched_barrier(0);
    });
    CODE_MARK(1);
    asm volatile("s_nop 7\n\ts_nop 1" : "+v"(acc[7]));
    ldsw(sm, pa + 7168, (u2){pk(lo2(acc[7])), pk(hi2(acc[7]))});
    asm volatile("" ::: "memory");
    ldsw(sm, pwv, 8u);
}

DI void role_ar(lds_t sm, const int slot, const unsigned nw, const APre& pre, Taps& t) {
    const int lane = lane_id(), col = lane & 15, g = lane >> 4;
    const int pw = A_CNT + slot * 32;
    f4 c[4];
    unsigned su0 = 0;
    su0 = taps_end<false>(sm, 0, pre.m[5], pw, t, c);
#pragma unroll
    for (int i = 0; i < 4; i++) ldsw(sm, A_ST + slot * A_STB + RB + 1024 * i + lane * 16, c[i]);
    asm volatile("" ::: "memory");
    ldsw(sm, pw + 4, 1u);
    __builtin_amdgcn_s_setprio(0);
    if (!__builtin_amdgcn_readfirstlane(su0)) lds_wait1(sm, pw + 8, 1);
    int bo = A_Y + 7168 + lane * 16;
    pin(bo);
    const u4 bd3 = ldsr<u4>(sm, bo), bd5 = ldsr<u4>(sm, bo + 8192), bd7 = ldsr<u4>(sm, bo + 16384), bd9 = ldsr<u4>(sm, bo + 24576);
    u4 aex = ldsr<u4>(sm, bo + 32768);
    lds_count<L_CNT>(sm);
    const int ub = A_U + slot * A_UST + 512 - 32 + col * 32 + g * 16;
    u4 w0[6], w1[6], q0, q1, q;
    w0[0] = ldsr<u4>(sm, ub); w1[0] = ldsr<u4>(sm, ub - 16); w0[1] = ldsr<u4>(sm, ub + 512); w1[1] = ldsr<u4>(sm, ub + 496);
    {
        const f4 b4 = mfma0(w0[0], bd3), b8 = mfma0(w0[0], bd5), b12 = mfma0(w1[0], bd3), b16 = mfma0(w1[0], bd5), c8 = mfma0(w0[0], bd7), c16 = mfma0(w1[0], bd9);
        q0 = (u4){pk(b4.y, b8.y), pk(b12.y, b16.y), pk(b4.x, b8.x), pk(b12.x, b16.x)};
        q1 = (u4){pk(b4.w, b8.w), pk(b12.w, b16.w), pk(b4.z, b8.z), pk(b12.z, b16.z)};
        q = (u4){pk(c8.w, c16.w), pk(c8.z, c16.z), pk(c8.y, c16.y), pk(c8.x, c16.x)};
    }
    pin(q0); pin(q1); pin(q);
    u4 k2[5][2], k3[8], edge;
    {
        unsigned t2 = (unsigned)(size_t)sm + A_Y + slot * 8192 + R_T2 + (col - 4 * g - 17) * 8;
        int t3 = A_Y + slot * 8192 + R_T3 + (col - 4 * g - 48) * 4;
        pin(t2); pin(t3);
        lds_wait1(sm, pw + 24, 1);
        __builtin_amdgcn_s_setprio(3);
        u2 a[6], b[6];
#pragma unroll
        for (int i = 0; i < 6; i++)
            asm volatile("ds_read_b64 %0, %2 offset:%3\n\tds_read_b64 %1, %2 offset:%4" : "=&v"(a[i]), "=&v"(b[i]) : "v"(t2), "i"(256 + 128 * (i >> 1) - 16 * (i & 1) - 8), "i"(256 + 128 * (i >> 1) - 16 * (i & 1)) : "memory");
        unsigned k1024 = ldsr<unsigned>(sm, A_Y + slot * 8192 + R_T2 + 372);
        __builtin_amdgcn_sched_barrier(0);
#pragma unroll
        for (int d = 7; d >= 4; d--)
            k3[d] = (u4){ldsr<unsigned>(sm, t3 + 64 * d - 12), ldsr<unsigned>(sm, t3 + 64 * d - 8), ldsr<unsigned>(sm, t3 + 64 * d - 4), ldsr<unsigned>(sm, t3 + 64 * d)};
        __builtin_amdgcn_sched_barrier(0);
        pin(k1024);
        __builtin_amdgcn_sched_barrier(0);
        aex &= __builtin_amdgcn_perm(k1024, k1024, 0x03020302u);
#pragma unroll
        for (int i = 0; i < 6; i++) k2[2 + (i >> 1)][i & 1] = (u4){a[i].x, a[i].y, b[i].x, b[i].y};
    }
    int pa = A_Y + slot * 8192 + 2048 + lane * 16;
    pin(pa);
    f4 acc[12];
    unsigned nread;
#pragma unroll
    for (int n = 0; n < 12; n++) acc[n] = (f4){0.f, 0.f, 0.f, 0.f};
    unsigned su, pr;
    int pwv = pw;
    pin(pwv);
#define SB __builtin_amdgcn_sched_barrier(0)
    static_for<0, 6>([&](auto NU) __attribute__((always_inline)) {
        constexpr int nu = NU.v;
        if (nu == 0 && slot < 2) __builtin_amdgcn_s_setprio(EP);
        if (nu == RT1 || (nu == 1 && slot < 2)) __builtin_amdgcn_s_setprio(2);
        if (nu == RT2) __builtin_amdgcn_s_setprio(1);
        if (nu == 5) CODE_MARK(2);
        f4 c8, c16;
        if constexpr (nu > 0) {
            const f4 b4 = mfma0(w0[nu], bd3); SB;
            ldsw(sm, pwv + 4, pr); asm volatile("" ::: "memory"); SB;
            const f4 b8 = mfma0(w0[nu], bd5); SB;
            if ((nu & 1) && nu < 5) { if (__builtin_expect(!__builtin_amdgcn_readfirstlane(su), 0)) lds_wait1(sm, pw + 10 + 2 * nu, 1); asm volatile("" ::: "memory"); }
            const f4 b12 = mfma0(w1[nu], bd3); SB;
            const f4 b16 = mfma0(w1[nu], bd5); SB;
            acc[nu + 2] = mfma(edge, w0[nu], acc[nu + 2]); SB;
            q0.x = pk(b4.y, b8.y); q0.z = pk(b4.x, b8.x); SB;
            if (nu < 4) { acc[nu + 4] = mfma(aex, w0[nu], acc[nu + 4]); SB; }
            q1.x = pk(b4.w, b8.w); q1.z = pk(b4.z, b8.z); SB;
            if (nu < 4) { c16 = mfma0(w1[nu], bd9); SB; }
            q0.y = pk(b12.y, b16.y); q0.w = pk(b12.x, b16.x); SB;
            if (nu < 4) { c8 = mfma0(w0[nu], bd7); SB; }
            q1.y = pk(b12.w, b16.w); q1.w = pk(b12.z, b16.z); SB;
            acc[nu + 2] = mfma(q0, k2[2][0], acc[nu + 2]); SB;
            if constexpr (nu < 5) { w0[nu + 1] = ldsr<u4>(sm, ub + 512 * (nu + 1)); w1[nu + 1] = ldsr<u4>(sm, ub + 512 * (nu + 1) - 16); SB; }
        } else {
            su = ldsr<unsigned>(sm, pwv + 24);
            acc[2] = mfma(q0, k2[2][0], acc[2]); acc[4] = mfma(aex, w0[0], acc[4]); SB;
        }
        if (nu < 5) { acc[nu + 3] = mfma(q0, k2[3][0], acc[nu + 3]); SB; }
        acc[nu + 2] = mfma(q1, k2[2][1], acc[nu + 2]); SB;
        if (nu < 4) { acc[nu + 4] = mfma(q0, k2[4][0], acc[nu + 4]); SB; }
        if (nu > 0 && nu < 4) { q.x = pk(c8.w, c16.w); q.y = pk(c8.z, c16.z); SB; }
        if (nu == 0) {
            if (__builtin_amdgcn_readfirstlane(su) < 2) lds_wait1(sm, pw + 24, 2);
            asm volatile("" ::: "memory");
            const u2 x = ldsr<u2>(sm, pre.m[0] + 512), y = ldsr<u2>(sm, pre.m[1] + 512);
            edge = (u4){x.x, x.y, y.x, y.y};
        }
        if ((nu & 1) == 0 && nu < 4) su = ldsr<unsigned>(sm, pwv + 12 + 2 * nu);
        if (nu == 4) nread = ldsr<unsigned>(sm, L_CNT);
        SB;
        if (nu < 5) { acc[nu + 3] = mfma(q1, k2[3][1], acc[nu + 3]); SB; }
        if (nu > 0 && nu < 4) { q.z = pk(c8.y, c16.y); q.w = pk(c8.x, c16.x); SB; }
        if (nu < 4) { acc[nu + 4] = mfma(q1, k2[4][1], acc[nu + 4]); SB; }
        if (nu < 4) {
#pragma unroll
            for (int d = 7; d >= 4; d--) if (nu + d < 8) acc[nu + d] = mfma(q, k3[d], acc[nu + d]);
        }
        if (nu == 0) acc[2] = mfma(edge, w0[0], acc[2]);
        pr = nu + 2; pin(pr); SB;
        if (nu == 5 && __builtin_expect(__builtin_amdgcn_readfirstlane(nread) < nw / 2, 0)) lds_wait1(sm, L_CNT, nw / 2);
        asm volatile("" ::: "memory");
        ldsw(sm, pa + 1024 * nu, acc[nu + 2]);
        asm volatile("" ::: "memory");
        SB;
    });
    ldsw(sm, pwv + 4, pr);
#undef SB
}

DI void role_aio(const Params& p, lds_t sm, const int j, const int cw, const int nch, const int rank) {
    const int lane = lane_id();
    __builtin_amdgcn_s_setprio(3);
    if (j) {
        ldsw(sm, A_U + (2 * j - 2 + (lane >> 5)) * A_UST + (lane & 31) * 16, (u4){0u, 0u, 0u, 0u});
        {
            const int o = A_NEAR + (2 * j - 2) * 4864 + (lane & 7) * 608 + (lane < 24 ? lane >> 3 : 63 + (lane >> 3)) * 8;
            ldsw(sm, o, (u2){0u, 0u}); ldsw(sm, o + 4864, (u2){0u, 0u});
            ldsw(sm, A_Y + (2 * j - 2) * 8192 + lane * 16, (u4){0u, 0u, 0u, 0u}); ldsw(sm, A_Y + (2 * j - 1) * 8192 + lane * 16, (u4){0u, 0u, 0u, 0u});
        }
        const int sh = 8 * (lane >> 4) - (lane & 15) + 16;
#pragma unroll 1
        for (int b = 3 - j; b < 5; b += 3) {
            const int lo = (0xC456B >> (4 * b)) & 15, len = (0x29753 >> (4 * b)) & 15;
            unsigned mk = (unsigned)((((1ull << len) - 1) << (lo + 16)) >> sh) & 0xffu;
            mk |= mk << 15;
            u4 f;
#pragma unroll
            for (int e = 0; e < 4; e++) f[e] = __umul24((mk >> (2 * e)) & 0x10001u, b == 4 ? 0xffffu : 0x3F80u);
            ldsw(sm, A_Y + b * 8192 + 7168 + lane * 16, f);
        }
        lds_count<L_CNT + 12>(sm);
    }
    if (j == 0) __builtin_amdgcn_s_sleep(UPD0);
    unsigned pf0 = 0, pf1 = 0;
    if (lane < 43) {
        pf0 = gload<unsigned>(p.u, (j * 512 + min(lane * 12 + (rank & 15), 511)) * 1152 + cw * 2);
        if (rank & 16) pf1 = gload<unsigned>(p.u, (j * 512 + min(lane * 12 + rank - 10, 511)) * 1152 + cw * 2);
    }
    if (j == 0) __builtin_amdgcn_s_sleep(DLYU);
    lds_wait1(sm, L_CNT + 8, j);
    u3 uin[8];
    const unsigned uo = lane * 4608 + j * (512 * 1152) + cw * 2;
#pragma unroll
    for (int i = 0; i < 8; i++) uin[i] = gload<u3>(p.u, uo + (i >> 2) * (256 * 1152), (i & 3) * 1152);
    asm volatile("" ::: "memory");
    ldsw(sm, L_CNT + 8, (unsigned)(j + 1));
    asm volatile("" :: "v"(pf0), "v"(pf1));
#pragma unroll
    for (int t = 0; t < 2; t++)
#pragma unroll
        for (int d = 0; d < 3; d++) {
            const int o = A_U + 2 * d * A_UST + 512 + 512 * t + 1024 * j + lane * 8;
            ldsw(sm, o, (u2){__builtin_amdgcn_perm(uin[4 * t + 1][d], uin[4 * t + 0][d], 0x05040100u), __builtin_amdgcn_perm(uin[4 * t + 3][d], uin[4 * t + 2][d], 0x05040100u)});
            ldsw(sm, o + A_UST, (u2){__builtin_amdgcn_perm(uin[4 * t + 1][d], uin[4 * t + 0][d], 0x07060302u), __builtin_amdgcn_perm(uin[4 * t + 3][d], uin[4 * t + 2][d], 0x07060302u)});
        }
    asm volatile("" ::: "memory");
    if (lane < 6) ldsw(sm, A_CNT + lane * 32 + 8 + j * 4, 1u);
    asm volatile("" ::: "memory");
    const rsrc_t ro = mkrsrc(p.out, 2048 * 2304);
    const int pg = A_CNT + max(5 - lane, 6 - nch) * 32, vo = ((lane & 15) * 16 + (lane >> 4) * 4) * 2304;
#pragma unroll 1
    for (int n = j; n < 8; n += 4) {
        u2 y[6];
#pragma unroll 1
        for (unsigned spin = 0; spin < (SPIN_CAP << 3); spin++) {
            asm volatile("" ::: "memory");
            const unsigned c = ldsr<unsigned>(sm, pg);
            if (__builtin_popcount((unsigned)__ballot(c <= (unsigned)n) & 63u) <= (n >= IOT ? IOK : 0)) break;
            if (__all(c + 1 >= (unsigned)n)) __builtin_amdgcn_s_sleep(IOS); else __builtin_amdgcn_s_sleep(6);
        }
#pragma unroll 1
        for (unsigned spin = 0; spin < (SPIN_CAP << 3); spin++) {
            unsigned c;
            asm volatile("ds_read_b32 %6, %8\n\tds_read_b64 %0, %7\n\tds_read_b64 %1, %7 offset:8192\n\tds_read_b64 %2, %7 offset:16384\n\tds_read_b64 %3, %7 offset:24576\n\t"
                         "ds_read_b64 %4, %7 offset:32768\n\tds_read_b64 %5, %7 offset:40960\n\ts_waitcnt lgkmcnt(0)"
                         : "=&v"(y[0]), "=&v"(y[1]), "=&v"(y[2]), "=&v"(y[3]), "=&v"(y[4]), "=&v"(y[5]), "=&v"(c)
                         : "v"((unsigned)(size_t)sm + A_Y + 1024 * n + lane * 16), "v"((unsigned)(size_t)sm + pg) : "memory");
            if (__all(c > (unsigned)n)) break;
        }
        CODE_MARK(3);
        const int so = n * (256 * 2304) + cw * 2;
        unsigned d[4][3];
#pragma unroll
        for (int i = 0; i < 4; i++)
#pragma unroll
            for (int k = 0; k < 3; k++) d[i][k] = __builtin_amdgcn_perm(y[2 * k + 1][i >> 1], y[2 * k][i >> 1], (i & 1) ? 0x07060302u : 0x05040100u);
        if (nch == 6) {
#pragma unroll
            for (int i = 0; i < 4; i++) __builtin_amdgcn_raw_buffer_store_b96((u3){d[i][0], d[i][1], d[i][2]}, ro, vo + (i & 1) * 2304, so + (i >> 1) * 4608, 0);
        } else {
#pragma unroll
            for (int i = 0; i < 4; i++) __builtin_amdgcn_raw_buffer_store_b64((u2){d[i][1], d[i][2]}, ro, vo + (i & 1) * 2304 + 4, so + (i >> 1) * 4608, 0);
        }
    }
}

DI void role_ac(const Params& p, lds_t sm, int slot, int half, const unsigned nw, const unsigned (&ld)[9]) {
    const int lane = lane_id(), col = lane & 15, g = lane >> 4;
    APre pre;
    const int st = A_ST + slot * A_STB + half * RB;
    pre.m[5] = st + col * 288 + g * 16;
    pre.m[0] = A_NEAR + slot * 4864 + 32 + (col & 7) * 608 + (col & 8) - 8 * g;
    pre.m[1] = pre.m[0] + ((col & 4) ? -4 * 608 : 4 * 608 - 8);
    pre.m[2] = A_NEAR + slot * 4864 + 16 + lane * 8;
    pre.m[3] = A_Y + slot * 8192 + (lane < 32 ? R_T2 + 120 + lane * 4 : R_T3 + lane * 2); pre.m[4] = pre.m[3] + (lane < 32 ? 2 : 64);
#pragma unroll
    for (int i = 0; i < 6; i++) pin(pre.m[i]);
    __builtin_amdgcn_s_setprio(2);
    Taps t;
    taps(sm, st, pre.m[5], ld, lo2f(ld[0]), t);
    unsigned dsk[1] = {ld[7]};
    pin(dsk[0]); pin(t.v[0]); pin(t.v[1]); pin(t.v[2]); pin(t.v[3]);
    asm volatile("" : "+s"(half), "+s"(slot));
    if (half) { role_ar(sm, slot, nw, pre, t); FINISH; kill(t.f); kill(t.v); kill(pre.m); kill(dsk); }
    asm volatile("" : "+s"(slot));
    role_an(sm, slot, pre, t, lo2f(dsk[0]));
}

constexpr Tab make_tab() {
    Tab a = {};
    auto roles = [](unsigned r0, unsigned r1, unsigned r2, unsigned r3) { return (r0 | r1 << 3 | r2 << 6 | r3 << 9) << 20; };
    unsigned s2 = 0;
    for (unsigned b = 0; b < 256; b++) {
        const unsigned x = b & 7, nb = x < 6 ? 20 : 14, q0 = b >> 3, q = q0 < 2 ? q0 : q0 < 34 - nb ? q0 + nb - 2 : q0 + nb - 32;
        if (q < nb) {
            unsigned k1 = 0, pi = 0;
            if (q < 2) { k1 = 2 * x + q; pi = 0; }
            else if (q < 10) { k1 = 2 * x; pi = q - 1; }
            else if (q < 18) { k1 = 2 * x + 1; pi = q < 14 ? 2 * (q - 10) + 1 : 2 * (q - 14) + 2; }
            else if (x < 4) { k1 = 13 + (x & 2); pi = 4 * (x & 1) + 2 * (q - 18) + 2; }
            else { k1 = 16; pi = 2 * (x - 4) + q - 18; }
            const bool rows = k1 < 16 && pi > 0;
            a.d[b][0] = rows ? s2 * 1152 : 0;
            a.d[b][1] = (rows ? roles(ROLE_B1, ROLE_B1, ROLE_B2, ROLE_B2) | s2 : roles(ROLE_NONE, ROLE_NONE, ROLE_B2, ROLE_B2) | 255u) | k1 << 8 | pi << 13;
            s2 += rows;
        } else {
            const unsigned m = q - nb, grp = m < 12 ? x * 12 + m : 96 + (x - 6) * 6 + m - 12, r = grp % 3, cw = 16 * (grp / 3) + (r == 2 ? 10 : 6 * r);
            a.d[b][0] = cw * 256;
            a.d[b][1] = roles(ROLE_IO, ROLE_AC, r == 2 ? ROLE_NONE : ROLE_AC, ROLE_AC) | cw | (r == 2 ? 4u : 6u) << 10 | (m < 12 ? m : m + 4) << 13;
        }
    }
    return a;
}
__device__ __constant__ const Tab role_tab = make_tab();

__global__ void __launch_bounds__(NTHR, 4) fused_kernel(Params) {
    __shared__ __attribute__((aligned(16))) char smem[LDS_BYTES];
    lds_t sm = (lds_t)smem;
    const int lane = threadIdx.x & 63;
    int w = __builtin_amdgcn_readfirstlane(threadIdx.x >> 6);
    u16v k0; u8v k1; u4 k2; u2 kd;
    asm volatile("s_load_dwordx2 %3, %6, %5\n\ts_load_dwordx16 %0, %4, 0x0\n\ts_load_dwordx8 %1, %4, 0x40\n\ts_load_dwordx4 %2, %4, 0x60"
                 : "=&s"(k0), "=&s"(k1), "=&s"(k2), "=&s"(kd) : "s"(__builtin_amdgcn_kernarg_segment_ptr()), "s"(blockIdx.x * 8), "s"(&role_tab));
    if (blockIdx.x < 16 && w < 8) {
        u64 pc; int len;
        asm volatile("s_getpc_b64 %0\n.Lfk_m0:\n\ts_max_i32 %1, 0, .Lfk_m1-.Lfk_m0\n\ts_max_i32 %1, %1, .Lfk_m2-.Lfk_m0\n\ts_max_i32 %1, %1, .Lfk_m3-.Lfk_m0\n\t"
                     "s_max_i32 %1, %1, .Lfk_m4-.Lfk_m0\n\ts_max_i32 %1, %1, .Lfk_m5-.Lfk_m0" : "=s"(pc), "=s"(len) :: "scc");
        const u4 rs = {(unsigned)pc, (unsigned)(pc >> 32) & 0xffffu, (unsigned)len, 0x00020000u};
        unsigned o = threadIdx.x * 128;
        asm volatile("s_nop 4\n\tbuffer_load_dword %0, %0, %1, 0 offen\n\ts_endpgm" : "+v"(o) : "s"(rs));
    }
    if (w == 8) ldsw(sm, L_CNT + lane * 4, 0u);
    int slot = (0x543254103210ull >> (4 * w - 16)) & 7, half = (0x0CF0 >> w) & 1;
    unsigned vb = (threadIdx.x >> 5) * (128 * 1152) + (lane & 31) * 4, va = slot * 256 + half * 128 + lane * 2;
    int rsh = 20 + 3 * (w >> 2);
    pin(vb); pin(va);
    asm volatile("s_waitcnt lgkmcnt(0)" : "+s"(k0), "+s"(k1), "+s"(k2), "+s"(kd), "+s"(slot), "+s"(half), "+s"(rsh) :: "memory");
    auto arg = [](unsigned lo, unsigned hi) { return (u16*)(((u64)hi << 32) | lo); };
    const Params p = {arg(k0[0], k0[1]), arg(k0[2], k0[3]), arg(k0[4], k0[5]), arg(k0[6], k0[7]), arg(k0[8], k0[9]), arg(k0[10], k0[11]), arg(k0[12], k0[13]), arg(k0[14], k0[15]),
                      arg(k1[0], k1[1]), arg(k1[2], k1[3]), arg(k1[4], k1[5]), arg(k1[6], k1[7]), arg(k2[0], k2[1]), (char*)arg(k2[2], k2[3])};
    unsigned ld[9];
    kill(ld);
    int role = (kd.y >> rsh) & 7;
    if (role == ROLE_AC) {
        const int up = w < 8 ? (~kd.y >> 10) & 2 : 0;
        slot += up;
        const unsigned o = va + (kd.x + up * 256);
        ld[0] = gload<u16>(p.logdt, (o >> 8) * 2);
        __builtin_amdgcn_sched_barrier(0);
        ld[1] = gload<u16>(p.are, o); ld[2] = gload<u16>(p.aim, o); ld[3] = gload<u16>(p.bre, o);
        ld[4] = gload<u16>(p.bim, o); ld[5] = gload<u16>(p.cre, o); ld[6] = gload<u16>(p.cim, o);
        ld[7] = gload<u16>(p.dskip, (o >> 8) * 2);
    }
    asm volatile("" : "+s"(role));
    if (role == ROLE_B1) {
#pragma unroll
        for (int j = 0; j < 9; j++) ld[j] = gload<unsigned>(p.x, vb + kd.x, j * 128);
    }
    asm volatile("" : "+s"(role));
    asm volatile("s_barrier" ::: "memory");
    if (role == ROLE_B2 && w >= 12 && (kd.y & 128u) == 0) {
        if (CD) __builtin_amdgcn_s_sleep(CD);
        const rsrc_t rC = mkrsrc(p.ws + WS_E + (kd.y & 255u) * SLOT_ROW, 146 * 128);
#pragma unroll
        for (int i = 0; i < 5; i++) __builtin_amdgcn_raw_buffer_store_b128((u4){~0u, ~0u, ~0u, ~0u}, rC, (threadIdx.x - 768) * 16, i * 4096, AUX_SC1);
    }
    asm volatile("" : "+s"(role));
    if (role == ROLE_NONE) FINISH;
    asm volatile("" : "+s"(role) :: "memory");
    if (role <= ROLE_B2) {
        const int s2 = kd.y & 255u;
        __builtin_amdgcn_s_setprio(3);
        u64 ep[1];
        kill(ep);
        if ((w >> 1) == 4) {
            u2 e;
            asm volatile("global_load_dwordx2 %0, %1, %2 sc1" : "=&v"(e) : "v"(0u), "s"(p.ws) : "memory");
            ep[0] = __builtin_bit_cast(u64, e);
        }
        asm volatile("" : "+s"(w));
        if (w < 12) b_tables(sm, w, s2);
        asm volatile("" : "+s"(role));
        if (role == ROLE_B1) { role_b1(p, sm, w, s2, ld); FINISH; kill(ep); }
        role_b2(p, sm, w - 8, s2 == 255 ? -1 : s2, (kd.y >> 8) & 31u, (kd.y >> 13) & 15u, ep[0]);
        FINISH; kill(ld);
    }
    asm volatile("" : "+s"(role));
    if (role == ROLE_AC) { role_ac(p, sm, slot, half, (kd.y >> 9) & 14u, ld); FINISH; }
    role_aio(p, sm, w, kd.y & 1023u, (kd.y >> 10) & 7u, (kd.y >> 13) & 31u);
    FINISH;
}

extern "C" void kernel_launch(void* const* d_in, const int* in_sizes, int n_in, void* d_out, int out_size, void* d_ws, size_t ws_size,
                              hipStream_t stream) {
    Params p;
    p.u = (const u16*)d_in[0]; p.are = (const u16*)d_in[1]; p.aim = (const u16*)d_in[2]; p.bre = (const u16*)d_in[3]; p.bim = (const u16*)d_in[4];
    p.cre = (const u16*)d_in[5]; p.cim = (const u16*)d_in[6]; p.logdt = (const u16*)d_in[7]; p.dskip = (const u16*)d_in[8];
    p.x = (const u16*)d_in[9]; p.wr = (const u16*)d_in[10]; p.wi = (const u16*)d_in[11];
    p.out = (u16*)d_out; p.ws = (char*)d_ws;
    fused_kernel<<<256, NTHR, 0, stream>>>(p);
}
```
